```python
import torch

def _softmax(x, axis=-1):
    return torch.softmax(x, dim=axis)

def _gelu(x):
    return torch.nn.functional.gelu(x, approximate='tanh')

def _silu(x):
    return torch.nn.functional.silu(x)

def _sigmoid(x):
    return torch.sigmoid(x)

def _layernorm(x, axis=-1, eps=1e-05):
    m = x.mean(dim=axis, keepdim=True)
    v = x.var(dim=axis, keepdim=True, unbiased=False)
    return (x - m) / torch.sqrt(v + eps)

def _scatter_add(tgt, idx, src, axis=0):
    return tgt.clone().index_add_(axis, idx, src)

def _segment_sum(src, seg, n):
    out = torch.zeros((n,) + src.shape[1:], dtype=src.dtype)
    return out.index_add_(0, seg, src)

def _pad(x, pw):
    flat = tuple((v for pair in reversed(pw) for v in pair))
    return torch.nn.functional.pad(x, flat)
kernel_name = 'adaLN_cond__spectral_gfnet__mixer_channel_mlp__prompt'

def setup_inputs():

    _w = lambda a: (lambda t: t.to(torch.bfloat16) if t.is_floating_point() else t)(torch.as_tensor(a))

    def _A_setup():
        B, S, D = (1, 2048, 1024)
        Df = D // 2 + 1
        return {'x': _w((lambda *s: torch.randn(*s, dtype=torch.bfloat16) * 0.05)(B, S, D)), 'w_r': _w((lambda *s: torch.randn(*s, dtype=torch.bfloat16) * 1.6)(S, Df) * 0.02), 'w_i': _w((lambda *s: torch.randn(*s, dtype=torch.bfloat16) * 1.6)(S, Df) * 0.02)}
    _A_in = _A_setup()

    def _B_setup():
        B, S, D, F = (1, 2048, 1024, 3584)
        return {'x': _w((lambda *s: torch.randn(*s, dtype=torch.bfloat16) * 0.05)(B, S, D)), 'w1': _w((lambda *s: torch.randn(*s, dtype=torch.bfloat16) * 3.2)(D, F) * D ** (-0.5)), 'b1': _w((lambda *s: torch.randn(*s, dtype=torch.bfloat16) * 0.8)(F)), 'w2': _w((lambda *s: torch.randn(*s, dtype=torch.bfloat16) * 0.05)(F, D) * F ** (-0.5)), 'b2': _w((lambda *s: torch.randn(*s, dtype=torch.bfloat16) * 0.1)(D))}
    _B_in = _B_setup()
    return {'A_x': _A_in['x'], 'A_w_r': _A_in['w_r'], 'A_w_i': _A_in['w_i'], 'B_x': _B_in['x'], 'B_w1': _B_in['w1'], 'B_b1': _B_in['b1'], 'B_w2': _B_in['w2'], 'B_b2': _B_in['b2'], 'c': _w((lambda *s: torch.randn(*s, dtype=torch.bfloat16) * 0.2)(1, 1024) * 8.0), 'w_mod': _w((lambda *s: torch.randn(*s, dtype=torch.bfloat16) * 0.2)(1024, 6 * 1024) * 1024 ** (-0.5) * 8.0)}

def reference(A_x, A_w_r, A_w_i, B_x, B_w1, B_b1, B_w2, B_b2, c, w_mod):

    def _A_ref(x, w_r, w_i):
        B, S, D = x.shape
        X = (lambda x, axes=(-2, -1): torch.fft.rfft2(x.float(), dim=axes))(x, axes=(-2, -1))
        W = w_r + 1j * w_i
        X = X * W
        y = (lambda x, s, axes=(-2, -1): torch.fft.irfft2(x, s=s, dim=axes).to(torch.bfloat16))(X, (S, D), axes=(-2, -1))
        return y

    def _B_ref(x, w1, b1, w2, b2):
        y = _layernorm(x, axis=-1)
        h = _gelu(torch.einsum('bsd,df->bsf', y, w1) + b1)
        return torch.einsum('bsf,fd->bsd', h, w2) + b2
    x = A_x
    mod = (_silu(c) @ w_mod).reshape(c.shape[0], 6, 1, -1)
    sa, ca, ga, sb, cb, gb = (mod[:, 0].clone(), mod[:, 1].clone(), mod[:, 2].clone(), mod[:, 3].clone(), mod[:, 4].clone(), mod[:, 5].clone())
    h = _layernorm(x, axis=-1) * (1 + ca) + sa
    x = x + ga * _A_ref(h, A_w_r, A_w_i)
    h = _layernorm(x, axis=-1) * (1 + cb) + sb
    x = x + gb * _B_ref(h, B_w1, B_b1, B_w2, B_b2)
    return x
```

```cpp
#include <hip/hip_runtime.h>
#include <hip/hip_bf16.h>

typedef unsigned short u16;
typedef unsigned int u32;
typedef float f32x2 __attribute__((ext_vector_type(2)));
typedef float f32x4 __attribute__((ext_vector_type(4)));
typedef float f32x16 __attribute__((ext_vector_type(16)));
typedef __bf16 bf16x2 __attribute__((ext_vector_type(2)));
typedef __bf16 bf16x8 __attribute__((ext_vector_type(8)));
typedef short s16x4 __attribute__((ext_vector_type(4)));
typedef u32 u32x2 __attribute__((ext_vector_type(2)));
typedef u32 u32x4 __attribute__((ext_vector_type(4)));
#define LAS __attribute__((address_space(3)))
#define DI __device__ __forceinline__

constexpr int S_ = 2048, D_ = 1024, F_ = 3584, DF_ = 513;

constexpr size_t WS_MOD = 0;
constexpr size_t WS_P1 = 1 << 20;
constexpr size_t WS_P2 = WS_P1 + (4 << 20);
constexpr size_t WS_P2X = WS_P2 + (4 << 20);
constexpr size_t WS_Y2 = WS_P2X + (1 << 20);
constexpr size_t WS_X1 = WS_Y2 + (4 << 20);
constexpr size_t WS_H = WS_X1 + (4 << 20);

DI u32 pk_bf16(float lo, float hi) {
  f32x2 v = {lo, hi};
  return __builtin_bit_cast(u32, __builtin_convertvector(v, bf16x2));
}
DI float bf_lo(u32 w) { return __builtin_bit_cast(float, w << 16); }
DI float bf_hi(u32 w) { return __builtin_bit_cast(float, w & 0xffff0000u); }
DI float round_bf(float x) { return bf_lo(pk_bf16(x, x)); }
DI float sin_rev(float r) { return __builtin_amdgcn_sinf(r); }
DI float cos_rev(float r) { return __builtin_amdgcn_cosf(r); }
DI f32x4 mfma16(u32x4 a, u32x4 b, f32x4 c) {
  return __builtin_amdgcn_mfma_f32_16x16x32_bf16(__builtin_bit_cast(bf16x8, a), __builtin_bit_cast(bf16x8, b), c, 0, 0, 0);
}
DI f32x16 mfma32(u32x4 a, u32x4 b, f32x16 c) {
  return __builtin_amdgcn_mfma_f32_32x32x16_bf16(__builtin_bit_cast(bf16x8, a), __builtin_bit_cast(bf16x8, b), c, 0, 0, 0);
}
template <int CTRL> DI float dpp_mov(float x) {
  return __builtin_bit_cast(float, __builtin_amdgcn_update_dpp(0, __builtin_bit_cast(int, x), CTRL, 0xf, 0xf, true));
}
DI float swap32_add(float a, float b) {
  auto r = __builtin_amdgcn_permlane32_swap(__builtin_bit_cast(u32, a), __builtin_bit_cast(u32, b), false, false);
  return __builtin_bit_cast(float, (u32)r[0]) + __builtin_bit_cast(float, (u32)r[1]);
}
DI float swap16_add(float a, float b) {
  auto r = __builtin_amdgcn_permlane16_swap(__builtin_bit_cast(u32, a), __builtin_bit_cast(u32, b), false, false);
  return __builtin_bit_cast(float, (u32)r[0]) + __builtin_bit_cast(float, (u32)r[1]);
}
DI float row_allsum(float v) {
  v += dpp_mov<0x128>(v); v += dpp_mov<0x124>(v); v += dpp_mov<0x122>(v); v += dpp_mov<0x121>(v);
  return v;
}
#define LDS_BARRIER() do { asm volatile("s_waitcnt lgkmcnt(0)" ::: "memory"); __builtin_amdgcn_s_barrier(); asm volatile("" ::: "memory"); } while (0)

DI void ln_stats8(const float (&v)[8][4], LAS float* red, int lane, int wave, float (&rs)[8], float (&nm)[8]) {
  float p[16];
#pragma unroll
  for (int j = 0; j < 8; ++j) {
    p[j] = (v[j][0] + v[j][1]) + (v[j][2] + v[j][3]);
    p[j + 8] = (v[j][0] * v[j][0] + v[j][1] * v[j][1]) + (v[j][2] * v[j][2] + v[j][3] * v[j][3]);
  }
#pragma unroll
  for (int i = 0; i < 8; ++i) p[i] = swap32_add(p[i], p[i + 8]);
#pragma unroll
  for (int i = 0; i < 4; ++i) p[i] = swap16_add(p[i], p[i + 4]);
#pragma unroll
  for (int i = 0; i < 4; ++i) p[i] = row_allsum(p[i]);
  if ((lane & 15) == 0) *(LAS f32x4*)(red + wave * 16 + (lane >> 4) * 4) = (f32x4){p[0], p[1], p[2], p[3]};
  LDS_BARRIER();
  float t = red[lane];
  t = swap32_add(t, t);
  t = swap16_add(t, t);
  float o = dpp_mov<0x128>(t);
  float mean = t * (1.0f / 1024.0f);
  float var = fmaxf(o * (1.0f / 1024.0f) - mean * mean, 0.0f);
  float r = __builtin_amdgcn_rsqf(var + 1e-5f);
  float m = -mean * r;
#pragma unroll
  for (int j = 0; j < 8; ++j) {
    rs[j] = __builtin_bit_cast(float, __builtin_amdgcn_readlane(__builtin_bit_cast(int, r), j));
    nm[j] = __builtin_bit_cast(float, __builtin_amdgcn_readlane(__builtin_bit_cast(int, m), j));
  }
}

DI f32x4 load_mod4(const float* part, int idx) {
  f32x4 a = *(const f32x4*)(part + idx), b = *(const f32x4*)(part + 6144 + idx);
  f32x4 c = *(const f32x4*)(part + 2 * 6144 + idx), d = *(const f32x4*)(part + 3 * 6144 + idx);
  f32x4 s = (a + b) + (c + d);
  return (f32x4){round_bf(s[0]), round_bf(s[1]), round_bf(s[2]), round_bf(s[3])};
}

__global__ __launch_bounds__(256) void k_mod(const u16* __restrict__ c, const u16* __restrict__ wm, float* __restrict__ part) {
  __shared__ float red[16 * 32];
  const int b = blockIdx.x, t = threadIdx.x;
  const int x = b & 7, y = b >> 3, z = y >> 1;
  const int cg = 2 * (x + 8 * (z % 12)) + (y & 1), sp = z / 12;
  const int row0 = sp * 256 + (t >> 2);
  u32x4 w[4];
  float cv[4];
#pragma unroll
  for (int p = 0; p < 4; ++p) w[p] = *(const u32x4*)(wm + (size_t)(row0 + 64 * p) * 6144 + cg * 32 + (t & 3) * 8);
#pragma unroll
  for (int p = 0; p < 4; ++p) {
    float v = bf_lo(c[row0 + 64 * p]);
    cv[p] = round_bf(v * __builtin_amdgcn_rcpf(1.0f + __builtin_amdgcn_exp2f(-1.44269504f * v)));
  }
  float acc[8];
#pragma unroll
  for (int i = 0; i < 8; ++i) acc[i] = 0.f;
#pragma unroll
  for (int p = 0; p < 4; ++p)
#pragma unroll
    for (int i = 0; i < 4; ++i) {
      acc[2 * i] += cv[p] * bf_lo(w[p][i]);
      acc[2 * i + 1] += cv[p] * bf_hi(w[p][i]);
    }
#pragma unroll
  for (int i = 0; i < 8; ++i) { acc[i] += dpp_mov<0x128>(acc[i]); acc[i] += dpp_mov<0x124>(acc[i]); }
  if ((t & 15) < 4) {
    float* r = red + (t >> 4) * 32 + (t & 3) * 8;
    *(f32x4*)r = (f32x4){acc[0], acc[1], acc[2], acc[3]};
    *(f32x4*)(r + 4) = (f32x4){acc[4], acc[5], acc[6], acc[7]};
  }
  __syncthreads();
  if (t < 32) {
    float s = 0.f;
#pragma unroll
    for (int i = 0; i < 16; ++i) s += red[i * 32 + t];
    part[sp * 6144 + cg * 32 + t] = s;
  }
}

template <int SGN> DI void dft32_matrix(int lane, u32x4 (&mre)[4], u32x4 (&mim)[4]) {
  const int x = lane & 31, h = lane >> 5;
#pragma unroll
  for (int g = 0; g < 4; ++g)
#pragma unroll
    for (int i = 0; i < 4; ++i) {
      float r = (float)((x * (8 * g + 4 * h + i)) & 31) * (1.0f / 32.0f);
      float cs = cos_rev(r), sn = SGN * sin_rev(r);
      mre[g][i] = pk_bf16(cs, -sn);
      mim[g][i] = pk_bf16(sn, cs);
    }
}
template <int SGN> DI void twiddle1024(int lane, float (&tc)[16], float (&ts)[16]) {
  const int x = lane & 31, h = lane >> 5;
#pragma unroll
  for (int r = 0; r < 16; ++r) {
    float a = (float)(x * (8 * (r >> 2) + 4 * h + (r & 3))) * (1.0f / 1024.0f);
    tc[r] = cos_rev(a); ts[r] = SGN * sin_rev(a);
  }
}
DI int pos0(int kd) {
  int k = kd < 512 ? kd : ((1024 - kd) & 511);
  return 4 * (k >> 1) + (k & 1) + (kd < 512 ? 0 : 2);
}

__global__ __launch_bounds__(256) void k_fft1(const u16* __restrict__ x, const float* __restrict__ part, u32* __restrict__ P1) {
  __shared__ __attribute__((aligned(16))) u32 sm[4 * 1024 + 64];
  LAS u32* img = (LAS u32*)sm;
  LAS float* red = (LAS float*)(sm + 4096);
  const int s2 = blockIdx.x, t = threadIdx.x, lane = t & 63, wave = __builtin_amdgcn_readfirstlane(t >> 6);
  u32x2 xr[8];
#pragma unroll
  for (int j = 0; j < 8; ++j) xr[j] = *(const u32x2*)(x + (size_t)(s2 + 256 * j) * D_ + 4 * t);
  f32x4 sa = load_mod4(part, 4 * t), ca = load_mod4(part, 1024 + 4 * t);
  u32x4 mre[4], mim[4];
  float tc[16], ts[16];
  dft32_matrix<-1>(lane, mre, mim);
  twiddle1024<-1>(lane, tc, ts);
  float v[8][4];
#pragma unroll
  for (int j = 0; j < 8; ++j) { v[j][0] = bf_lo(xr[j][0]); v[j][1] = bf_hi(xr[j][0]); v[j][2] = bf_lo(xr[j][1]); v[j][3] = bf_hi(xr[j][1]); }
  float rs[8], nm[8];
  ln_stats8(v, red, lane, wave, rs, nm);
  u32x4 o[4];
#pragma unroll
  for (int e = 0; e < 4; ++e) {
    float n[8];
#pragma unroll
    for (int j = 0; j < 8; ++j) n[j] = v[j][e] * rs[j] + nm[j];
    const float g = round_bf(1.0f + ca[e]);
    float a0 = n[0] + n[4], a1 = n[0] - n[4], a2 = n[2] + n[6], a3 = n[2] - n[6];
    float a4 = n[1] + n[5], a5 = n[1] - n[5], a6 = n[3] + n[7], a7 = n[3] - n[7];
    float b0 = a0 + a2, b1 = a0 - a2, b2 = a4 + a6, b3 = a6 - a4;
    float c0 = (a5 - a7) * 0.70710678f, c1 = (a5 + a7) * 0.70710678f;
    o[0][e] = pk_bf16((b0 + b2) * g + 8.0f * sa[e], (b0 - b2) * g);
    o[1][e] = pk_bf16((a1 + c0) * g, (-a3 - c1) * g);
    o[2][e] = pk_bf16(b1 * g, b3 * g);
    o[3][e] = pk_bf16((a1 - c0) * g, (a3 - c1) * g);
  }
#pragma unroll
  for (int c = 0; c < 4; ++c) *(LAS u32x4*)(img + c * 1024 + 4 * t) = o[c];
  LDS_BARRIER();
  u32x4 d[4];
  {
    LAS u32* src = img + wave * 1024 + (lane & 31) + 128 * (lane >> 5);
#pragma unroll
    for (int g = 0; g < 4; ++g)
#pragma unroll
      for (int i = 0; i < 4; ++i) d[g][i] = src[32 * (8 * g + i)];
  }
  f32x16 re = {}, im = {};
#pragma unroll
  for (int g = 0; g < 4; ++g) { re = mfma32(d[g], mre[g], re); im = mfma32(d[g], mim[g], im); }
#pragma unroll
  for (int r = 0; r < 16; ++r) d[r >> 2][r & 3] = pk_bf16(re[r] * tc[r] - im[r] * ts[r], re[r] * ts[r] + im[r] * tc[r]);
  re = (f32x16){}; im = (f32x16){};
#pragma unroll
  for (int g = 0; g < 4; ++g) { re = mfma32(mre[g], d[g], re); im = mfma32(mim[g], d[g], im); }
  u32* dst = P1 + ((size_t)wave * 256 + s2) * 1024;
  const int kb = lane & 31, h = lane >> 5;
  if (wave == 0) {
#pragma unroll
    for (int r = 0; r < 16; ++r) dst[pos0(kb + 32 * (8 * (r >> 2) + 4 * h + (r & 3)))] = pk_bf16(re[r], im[r]);
  } else {
#pragma unroll
    for (int r = 0; r < 16; ++r) dst[kb + 32 * (8 * (r >> 2) + 4 * h + (r & 3))] = pk_bf16(re[r], im[r]);
  }
}

struct Mat16 { u32x4 re, im; };
template <int SGN, bool K1_ON_N> DI Mat16 dft16_matrix(int lane, int k1) {
  const int x = lane & 15, q = lane >> 4;
  Mat16 m;
#pragma unroll
  for (int i = 0; i < 4; ++i) {
    const int n = 4 * q + i;
    float r = (float)((8 * x * n + k1 * (K1_ON_N ? n : x)) & 127) * (1.0f / 128.0f);
    float cs = cos_rev(r), sn = SGN * sin_rev(r);
    m.re[i] = pk_bf16(cs, -sn);
    m.im[i] = pk_bf16(sn, cs);
  }
  return m;
}
struct Tw4 { float c[4], s[4]; };
template <int SGN, bool J_ON_N> DI Tw4 twiddle2048(int lane, int k1, float off, float amp) {
  const int x = lane & 15, q = lane >> 4;
  Tw4 t;
#pragma unroll
  for (int i = 0; i < 4; ++i) {
    const int n = 4 * q + i, j = J_ON_N ? n : x, ka = J_ON_N ? x : n;
    float r = (float)(j * (8 * ka + k1)) * (1.0f / 2048.0f) + off;
    t.c[i] = amp * cos_rev(r); t.s[i] = amp * SGN * sin_rev(r);
  }
  return t;
}
DI u32x4 cmul_pack(f32x4 re, f32x4 im, const float (&c)[4], const float (&s)[4]) {
  u32x4 o;
#pragma unroll
  for (int i = 0; i < 4; ++i) o[i] = pk_bf16(re[i] * c[i] - im[i] * s[i], re[i] * s[i] + im[i] * c[i]);
  return o;
}
DI void load_w(const u16* __restrict__ wr, const u16* __restrict__ wi, int ks, int kd, float& c, float& s) {
  const int ksm = (2048 - ks) & 2047;
  if (kd == 0 || kd == 512) {
    c = 0.5f * (bf_lo(wr[ks * DF_ + kd]) + bf_lo(wr[ksm * DF_ + kd]));
    s = 0.5f * (bf_lo(wi[ks * DF_ + kd]) - bf_lo(wi[ksm * DF_ + kd]));
  } else if (kd < 512) {
    c = bf_lo(wr[ks * DF_ + kd]); s = bf_lo(wi[ks * DF_ + kd]);
  } else {
    c = bf_lo(wr[ksm * DF_ + 1024 - kd]); s = -bf_lo(wi[ksm * DF_ + 1024 - kd]);
  }
}
DI void fft2_tail(f32x4& re, f32x4& im, const Tw4& twf, const Mat16& f16, const Tw4& w, const Mat16& f16c, const Tw4& twi, const Mat16& fat) {
  const f32x4 z = {};
  u32x4 d = cmul_pack(re, im, twf.c, twf.s);
  re = mfma16(f16.re, d, z); im = mfma16(f16.im, d, z);
  d = cmul_pack(re, im, w.c, w.s);
  re = mfma16(d, f16c.re, z); im = mfma16(d, f16c.im, z);
  d = cmul_pack(re, im, twi.c, twi.s);
  re = mfma16(fat.re, d, z); im = mfma16(fat.im, d, z);
}

__global__ __launch_bounds__(256) void k_fft2(const u32* __restrict__ P1, const u16* __restrict__ wr, const u16* __restrict__ wi,
                                               u32* __restrict__ P2, u32* __restrict__ P2X) {
  __shared__ __attribute__((aligned(16))) u32 sm[4096];
  LAS u32* img = (LAS u32*)sm;
  const int b = blockIdx.x, t = threadIdx.x, lane = t & 63, wave = __builtin_amdgcn_readfirstlane(t >> 6);
  const bool extra = (b == 256);
  const int cls = extra ? 0 : (b >> 6), tile = extra ? 0 : (8 * (b & 7) + ((b >> 3) & 7));
  {
    const u32* src = P1 + ((size_t)cls * 256 + wave * 64 + (lane >> 2)) * 1024 + tile * 16 + 4 * ((lane & 3) ^ ((lane >> 4) & 3));
#pragma unroll
    for (int p = 0; p < 4; ++p)
      __builtin_amdgcn_global_load_lds(src + (size_t)p * 16 * 1024, (LAS void*)(img + (wave * 4 + p) * 256), 16, 0, 0);
  }
  const int x = lane & 15, q = lane >> 4;
  const int ldsoff = (x + 64 * q) * 16 + 4 * ((wave ^ (x >> 2)) & 3);
  Tw4 w[4];
  const Mat16 f16 = dft16_matrix<-1, true>(lane, 0), f16c = dft16_matrix<1, true>(lane, 0);
  u32x4 out[4];
  if (cls != 0) {
    const int k1 = cls;
#pragma unroll
    for (int c = 0; c < 4; ++c)
#pragma unroll
      for (int i = 0; i < 4; ++i) load_w(wr, wi, k1 + 8 * x + 128 * (4 * q + i), tile * 16 + wave * 4 + c, w[c].c[i], w[c].s[i]);
    const Mat16 fa = dft16_matrix<-1, true>(lane, k1), fat = dft16_matrix<1, false>(lane, k1);
    const Tw4 twf = twiddle2048<-1, true>(lane, k1, 0.f, 1.f), twi = twiddle2048<1, false>(lane, k1, 0.f, 1.f);
    asm volatile("s_waitcnt vmcnt(0)" ::: "memory");
    LDS_BARRIER();
    u32x4 r[4];
#pragma unroll
    for (int i = 0; i < 4; ++i) r[i] = *(LAS u32x4*)(img + ldsoff + i * 256);
#pragma unroll
    for (int c = 0; c < 4; ++c) {
      const u32x4 d = {r[0][c], r[1][c], r[2][c], r[3][c]};
      f32x4 re = mfma16(d, fa.re, (f32x4){}), im = mfma16(d, fa.im, (f32x4){});
      fft2_tail(re, im, twf, f16, w[c], f16c, twi, fat);
#pragma unroll
      for (int i = 0; i < 4; ++i) out[i][c] = pk_bf16(re[i], im[i]);
    }
  } else {
    const int kd0 = extra ? 512 : (tile * 8 + wave * 2);
#pragma unroll
    for (int c = 0; c < 4; ++c)
#pragma unroll
      for (int i = 0; i < 4; ++i)
        load_w(wr, wi, (c & 1) * 4 + 8 * x + 128 * (4 * q + i), extra ? 512 : kd0 + (c >> 1), w[c].c[i], w[c].s[i]);
    const Mat16 fa4 = dft16_matrix<-1, true>(lane, 4), fat4 = dft16_matrix<1, false>(lane, 4);
    const Tw4 twf0 = twiddle2048<-1, true>(lane, 0, 0.f, 0.5f), twi0 = twiddle2048<1, false>(lane, 0, 0.f, 1.f);
    const Tw4 twf4 = twiddle2048<-1, true>(lane, 4, 0.25f, 0.5f), twi4 = twiddle2048<1, false>(lane, 4, 0.f, 1.f);
    asm volatile("s_waitcnt vmcnt(0)" ::: "memory");
    LDS_BARRIER();
    u32x4 r[4];
#pragma unroll
    for (int i = 0; i < 4; ++i) r[i] = *(LAS u32x4*)(img + ldsoff + i * 256);
    const bool self0 = (kd0 == 0);
#pragma unroll
    for (int p = 0; p < 2; ++p) {
      u32x4 da, db, dn;
#pragma unroll
      for (int i = 0; i < 4; ++i) {
        u32 a = r[i][p], m = r[i][p + 2];
        if (p == 0) { u32 a0 = a; a = extra ? m : a; m = self0 ? a0 : m; }
        da[i] = a; db[i] = m ^ 0x80000000u; dn[i] = m ^ 0x00008000u;
      }
      f32x4 re0 = mfma16(db, f16.re, mfma16(da, f16.re, (f32x4){})), im0 = mfma16(db, f16.im, mfma16(da, f16.im, (f32x4){}));
      f32x4 re4 = mfma16(dn, fa4.re, mfma16(da, fa4.re, (f32x4){})), im4 = mfma16(dn, fa4.im, mfma16(da, fa4.im, (f32x4){}));
      fft2_tail(re0, im0, twf0, f16, w[2 * p], f16c, twi0, f16c);
      fft2_tail(re4, im4, twf4, f16, w[2 * p + 1], f16c, twi4, fat4);
#pragma unroll
      for (int i = 0; i < 4; ++i) {
        out[i][p] = pk_bf16(re0[i] - im4[i], im0[i] + re4[i]);
        out[i][p + 2] = pk_bf16(re0[i] + im4[i], re4[i] - im0[i]);
      }
    }
  }
#pragma unroll
  for (int i = 0; i < 4; ++i) *(LAS u32x4*)(img + ldsoff + i * 256) = out[i];
  LDS_BARRIER();
  {
    const int row = wave * 64 + (lane >> 2), ch = 4 * ((lane & 3) ^ ((lane >> 4) & 3));
    u32* dst = extra ? (P2X + row * 16 + ch) : (P2 + ((size_t)cls * 256 + row) * 1024 + tile * 16 + ch);
    const size_t rstride = extra ? 16 * 16 : 16 * 1024;
#pragma unroll
    for (int p = 0; p < 4; ++p) *(u32x4*)(dst + p * rstride) = *(LAS u32x4*)(img + (wave * 4 + p) * 256 + lane * 4);
  }
}

__global__ __launch_bounds__(256) void k_fft3(const u32* __restrict__ P2, const u32* __restrict__ P2X, const u16* __restrict__ x,
                                               const float* __restrict__ part, u16* __restrict__ x1o, u16* __restrict__ y2o) {
  __shared__ __attribute__((aligned(16))) float sm[8 * 1024 + 128];
  LAS float* img = (LAS float*)sm;
  LAS float* red = img + 8192;
  const int s2 = blockIdx.x, t = threadIdx.x, lane = t & 63, wave = __builtin_amdgcn_readfirstlane(t >> 6);
  const int xl = lane & 31, h = lane >> 5;
  u32x4 d[4];
  {
    const u32* src = P2 + ((size_t)wave * 256 + s2) * 1024;
    if (wave == 0) {
#pragma unroll
      for (int r = 0; r < 16; ++r) {
        const int kd = xl + 32 * (8 * (r >> 2) + 4 * h + (r & 3));
        const u32* p = src + pos0(kd);
        if (r == 8) p = (kd == 512) ? (P2X + s2 * 16) : p;
        d[r >> 2][r & 3] = *p;
      }
    } else {
#pragma unroll
      for (int r = 0; r < 16; ++r) d[r >> 2][r & 3] = src[xl + 32 * (8 * (r >> 2) + 4 * h + (r & 3))];
    }
  }
  u32x2 xr[8];
#pragma unroll
  for (int j = 0; j < 8; ++j) xr[j] = *(const u32x2*)(x + (size_t)(s2 + 256 * j) * D_ + 4 * t);
  f32x4 ga = load_mod4(part, 2048 + 4 * t), sb = load_mod4(part, 3072 + 4 * t), cb = load_mod4(part, 4096 + 4 * t);
  u32x4 mre[4], mim[4];
  float tc[16], ts[16];
  dft32_matrix<1>(lane, mre, mim);
  twiddle1024<1>(lane, tc, ts);
  f32x16 re = {}, im = {};
#pragma unroll
  for (int g = 0; g < 4; ++g) { re = mfma32(d[g], mre[g], re); im = mfma32(d[g], mim[g], im); }
#pragma unroll
  for (int r = 0; r < 16; ++r) d[r >> 2][r & 3] = pk_bf16(re[r] * tc[r] - im[r] * ts[r], re[r] * ts[r] + im[r] * tc[r]);
  re = (f32x16){}; im = (f32x16){};
#pragma unroll
  for (int g = 0; g < 4; ++g) { re = mfma32(d[g], mre[g], re); im = mfma32(d[g], mim[g], im); }
#pragma unroll
  for (int g = 0; g < 4; ++g) {
    const int off = 32 * xl + 4 * ((2 * g + h) ^ (xl & 7));
    *(LAS f32x4*)(img + (2 * wave) * 1024 + off) = (f32x4){re[4 * g], re[4 * g + 1], re[4 * g + 2], re[4 * g + 3]};
    *(LAS f32x4*)(img + (2 * wave + 1) * 1024 + off) = (f32x4){im[4 * g], im[4 * g + 1], im[4 * g + 2], im[4 * g + 3]};
  }
  LDS_BARRIER();
  f32x4 u[8];
  {
    const int off = 4 * ((t & ~7) + ((t & 7) ^ ((t >> 3) & 7)));
#pragma unroll
    for (int p = 0; p < 8; ++p) u[p] = *(LAS f32x4*)(img + p * 1024 + off);
  }
  float v[8][4];
#pragma unroll
  for (int e = 0; e < 4; ++e) {
    const float u0 = u[0][e], u4 = u[1][e], r1 = u[2][e], i1 = u[3][e], r2 = u[4][e], i2 = u[5][e], r3 = u[6][e], i3 = u[7][e];
    const float e0 = u0 + u4, e1 = u0 - u4;
    const float p0 = e0 + 2.0f * r2, p2 = e0 - 2.0f * r2, p1 = e1 - 2.0f * i2, p3 = e1 + 2.0f * i2;
    const float q0 = 2.0f * (r1 + r3), q2 = 2.0f * (i3 - i1);
    const float A = r1 - r3, B = i1 + i3;
    const float q1 = 1.41421356f * (A - B), q3 = -1.41421356f * (A + B);
    const float gs = ga[e] * (1.0f / 2097152.0f);
    float y[8] = {p0 + q0, p1 + q1, p2 + q2, p3 + q3, p0 - q0, p1 - q1, p2 - q2, p3 - q3};
#pragma unroll
    for (int j = 0; j < 8; ++j) {
      const float xv = (e & 1) ? bf_hi(xr[j][e >> 1]) : bf_lo(xr[j][e >> 1]);
      v[j][e] = xv + gs * y[j];
    }
  }
#pragma unroll
  for (int j = 0; j < 8; ++j) {
    u32x2 o = {pk_bf16(v[j][0], v[j][1]), pk_bf16(v[j][2], v[j][3])};
    *(u32x2*)(x1o + (size_t)(s2 * 8 + j) * D_ + 4 * t) = o;
    v[j][0] = bf_lo(o[0]); v[j][1] = bf_hi(o[0]); v[j][2] = bf_lo(o[1]); v[j][3] = bf_hi(o[1]);
  }
  float rs[8], nm[8];
  ln_stats8(v, red, lane, wave, rs, nm);
#pragma unroll
  for (int e = 0; e < 4; ++e) {
    const float g = round_bf(1.0f + cb[e]);
#pragma unroll
    for (int j = 0; j < 8; ++j) v[j][e] = (v[j][e] * rs[j] + nm[j]) * g + sb[e];
  }
  ln_stats8(v, red + 64, lane, wave, rs, nm);
#pragma unroll
  for (int j = 0; j < 8; ++j) {
    u32x2 o = {pk_bf16(v[j][0] * rs[j] + nm[j], v[j][1] * rs[j] + nm[j]), pk_bf16(v[j][2] * rs[j] + nm[j], v[j][3] * rs[j] + nm[j])};
    *(u32x2*)(y2o + (size_t)(s2 * 8 + j) * D_ + 4 * t) = o;
  }
}

DI u32x2 tr_read(LAS char* p) {
  return __builtin_bit_cast(u32x2, __builtin_amdgcn_ds_read_tr16_b64_v4i16((LAS s16x4*)p));
}
template <int N> DI void wait_vm() { asm volatile("s_waitcnt vmcnt(%0)" ::"n"(N) : "memory"); }
#define RAW_BARRIER() do { asm volatile("" ::: "memory"); __builtin_amdgcn_s_barrier(); asm volatile("" ::: "memory"); } while (0)
DI void dma16(__amdgpu_buffer_rsrc_t rs, LAS char* dst, u32 voff, u32 soff) {
  __builtin_amdgcn_raw_ptr_buffer_load_lds(rs, (LAS void*)dst, 16, voff, soff, 0, 0);
}

constexpr int G1_STAGE = 16384 + 4 * 7168, G1_NS = 3, G1_NT = 16;
__global__ __launch_bounds__(512, 2) void k_gemm1(const u16* __restrict__ A, const u16* __restrict__ W, const u16* __restrict__ bias,
                                                   u16* __restrict__ H) {
  extern __shared__ __attribute__((aligned(16))) char smem[];
  LAS char* lds = (LAS char*)smem;
  const int b = blockIdx.x, lane = threadIdx.x & 63, wave = __builtin_amdgcn_readfirstlane(threadIdx.x >> 6);
  const int xcd = b & 7, y = b >> 3;
  const int tm = 4 * (xcd >> 1) + (y & 3), tn = 8 * (xcd & 1) + (y >> 2);
  const int col0 = 224 * tn, odd = tn & 1;
  const int gcol = col0 + (odd ? 32 : 0), hcol = col0 + (odd ? 0 : 192);
  if (wave >= 4) {
    const int p = wave - 4;
    const auto ra = __builtin_amdgcn_make_buffer_rsrc((void*)A, 0, S_ * D_ * 2, 0x00020000);
    const auto rb = __builtin_amdgcn_make_buffer_rsrc((void*)W, 0, D_ * F_ * 2, 0x00020000);
    const u32 r8 = lane >> 3;
    const u32 va0 = r8 * 2048 + (((lane & 7) ^ (r8 >> 1)) << 4), va1 = r8 * 2048 + (((lane & 7) ^ (4 + (r8 >> 1))) << 4);
    const u32 vb = (8 * (r8 >> 2) + (r8 & 3)) * (F_ * 2) + (((((lane & 7) >> 1) ^ (r8 >> 1)) << 5) | ((lane & 1) << 4));
    const u32 r16 = lane >> 2;
    const u32 vh = (8 * (r16 >> 2) + (r16 & 3)) * (F_ * 2) + (((((lane >> 1) & 1) ^ ((r16 >> 2) & 1)) << 5) | ((lane & 1) << 4));
    const u32 sa0 = (tm * 128 + p * 32) * 2048;
    const u32 sb0 = (32 * (p >> 1) + 4 * (p & 1)) * (F_ * 2);
    auto issue = [&](int t, int slot) {
      LAS char* st = lds + slot * G1_STAGE;
      const u32 sa = sa0 + t * 128, sb = sb0 + t * 64 * (F_ * 2);
#pragma unroll
      for (int i = 0; i < 4; ++i) dma16(ra, st + (4 * p + i) * 1024, (i & 1) ? va1 : va0, sa + i * 8 * 2048);
      LAS char* sbp = st + 16384 + p * 7168;
#pragma unroll
      for (int g = 0; g < 3; ++g)
#pragma unroll
        for (int hb = 0; hb < 2; ++hb) dma16(rb, sbp + g * 2048 + hb * 1024, vb, sb + hb * 16 * (F_ * 2) + (gcol + 64 * g) * 2);
      dma16(rb, sbp + 6144, vh, sb + hcol * 2);
    };
    issue(0, 0); issue(1, 1); issue(2, 2);
    wait_vm<22>();
    RAW_BARRIER();
    int slot = 0;
    for (int t = 0; t < G1_NT; ++t) {
      if (t < G1_NT - 2) wait_vm<11>(); else wait_vm<0>();
      RAW_BARRIER();
      if (t + 3 < G1_NT) issue(t + 3, slot);
      slot = (slot == 2) ? 0 : slot + 1;
    }
    return;
  }
  const int wm = wave >> 1, wn = wave & 1, fr = lane & 15, q = lane >> 4;
  u32 boff[7];
  int ncol[7];
  {
    const u32 row = (lane & 31) >> 2, f = row >> 1, base = 16384 + (lane >> 5) * 1024 + row * 128 + (lane & 3) * 8;
#pragma unroll
    for (int j = 0; j < 4; ++j) { boff[j] = base + wn * 4096 + ((j ^ f) << 5); ncol[j] = gcol + wn * 128 + 16 * j; }
#pragma unroll
    for (int j = 0; j < 2; ++j) { boff[4 + j] = base + 2048 + (((2 * wn + j) ^ f) << 5); ncol[4 + j] = gcol + 64 + 32 * wn + 16 * j; }
    boff[6] = 16384 + 6144 + (lane >> 2) * 64 + ((wn ^ (q & 1)) << 5) + (lane & 3) * 8;
    ncol[6] = hcol + 16 * wn;
  }
  u32 aoff[2];
  aoff[0] = (wm * 64 + fr) * 128 + ((q ^ (fr >> 1)) << 4);
  aoff[1] = aoff[0] ^ 64;
  f32x4 acc[4][7];
#pragma unroll
  for (int j = 0; j < 7; ++j) {
    const u32x2 bv = *(const u32x2*)(bias + ncol[j] + 4 * q);
    const f32x4 bf = {bf_lo(bv[0]), bf_hi(bv[0]), bf_lo(bv[1]), bf_hi(bv[1])};
#pragma unroll
    for (int i = 0; i < 4; ++i) acc[i][j] = bf;
  }
  u32x4 fa[2][4], fb[2][7];
  auto ldfrag = [&](int buf, u32 st, int s) {
#pragma unroll
    for (int i = 0; i < 4; ++i) fa[buf][i] = *(LAS u32x4*)(lds + st + aoff[s] + i * 2048);
#pragma unroll
    for (int j = 0; j < 7; ++j) {
      const u32x2 lo = tr_read(lds + st + boff[j] + (2 * s) * 7168), hi = tr_read(lds + st + boff[j] + (2 * s + 1) * 7168);
      fb[buf][j] = (u32x4){lo[0], lo[1], hi[0], hi[1]};
    }
  };
  auto mma = [&](int buf) {
#pragma unroll
    for (int j = 0; j < 7; ++j)
#pragma unroll
      for (int i = 0; i < 4; ++i) acc[i][j] = mfma16(fb[buf][j], fa[buf][i], acc[i][j]);
  };
  auto sched = [&]() {
#pragma unroll
    for (int i = 0; i < 18; ++i) { __builtin_amdgcn_sched_group_barrier(0x8, 1, 0); __builtin_amdgcn_sched_group_barrier(0x100, 1, 0); }
    __builtin_amdgcn_sched_group_barrier(0x8, 10, 0);
    __builtin_amdgcn_sched_barrier(0);
  };
  RAW_BARRIER();
  ldfrag(0, 0, 0);
  u32 st = 0;
  for (int t = 0; t < G1_NT; ++t) {
    ldfrag(1, st, 1);
    mma(0);
    sched();
    LDS_BARRIER();
    st = (st == 2 * G1_STAGE) ? 0 : st + G1_STAGE;
    ldfrag(0, st, 0);
    mma(1);
    sched();
  }
#pragma unroll
  for (int i = 0; i < 4; ++i) {
    u32x2 o[7];
#pragma unroll
    for (int j = 0; j < 7; ++j) {
      float r[4];
#pragma unroll
      for (int e = 0; e < 4; ++e) {
        const float v = acc[i][j][e];
        const float z = v * (v * v * -0.1029432397f + -2.302208198f);
        r[e] = v * __builtin_amdgcn_rcpf(1.0f + __builtin_amdgcn_exp2f(z));
      }
      o[j] = (u32x2){pk_bf16(r[0], r[1]), pk_bf16(r[2], r[3])};
    }
    u16* hb = H + ((size_t)(tm * 8 + wm * 4 + i) * 112) * 512 + lane * 8;
#pragma unroll
    for (int pj = 0; pj < 3; ++pj)
      *(u32x4*)(hb + (ncol[2 * pj] >> 5) * 512) = (u32x4){o[2 * pj][0], o[2 * pj][1], o[2 * pj + 1][0], o[2 * pj + 1][1]};
    *(u32x2*)(hb + (hcol >> 5) * 512 + wn * 4) = o[6];
  }
}

constexpr int G2_STAGE = 32768 + 16384, G2_NS = 3, G2_NT = 28;
__global__ __launch_bounds__(512, 2) void k_gemm2(const u16* __restrict__ H, const u16* __restrict__ W, const u16* __restrict__ bias,
                                                   const float* __restrict__ part, const u16* __restrict__ x1, u16* __restrict__ out) {
  extern __shared__ __attribute__((aligned(16))) char smem[];
  LAS char* lds = (LAS char*)smem;
  const int b = blockIdx.x, lane = threadIdx.x & 63, wave = __builtin_amdgcn_readfirstlane(threadIdx.x >> 6);
  const int xcd = b & 7, y = b >> 3;
  const int tm = 4 * (xcd >> 1) + (y & 3), tn = 8 * (xcd & 1) + (y >> 2);
  if (wave >= 4) {
    const int p = wave - 4;
    const auto ra = __builtin_amdgcn_make_buffer_rsrc((void*)H, 0, S_ * F_ * 2, 0x00020000);
    const auto rb = __builtin_amdgcn_make_buffer_rsrc((void*)W, 0, F_ * D_ * 2, 0x00020000);
    const u32 r8 = lane >> 3;
    const u32 va = lane * 16;
    const u32 vb = r8 * 2048 + (((((lane & 7) >> 1) ^ (r8 >> 1)) << 5) | ((lane & 1) << 4));
    const u32 sa0 = (tm * 8 + 2 * p) * 112 * 1024;
    const u32 sb0 = p * 32 * 2048 + tn * 128;
    auto issue = [&](int t, int slot) {
      LAS char* st = lds + slot * G2_STAGE;
      const u32 sa = sa0 + t * 4096, sb = sb0 + t * 128 * 2048;
#pragma unroll
      for (int i = 0; i < 8; ++i) dma16(ra, st + (8 * p + i) * 1024, va, sa + (i >> 2) * 112 * 1024 + (i & 3) * 1024);
#pragma unroll
      for (int i = 0; i < 4; ++i) dma16(rb, st + 32768 + (4 * p + i) * 1024, vb, sb + i * 8 * 2048);
    };
    issue(0, 0); issue(1, 1); issue(2, 2);
    wait_vm<24>();
    RAW_BARRIER();
    int slot = 0;
    for (int t = 0; t < G2_NT; ++t) {
      if (t < G2_NT - 2) wait_vm<12>(); else wait_vm<0>();
      RAW_BARRIER();
      if (t + 3 < G2_NT) issue(t + 3, slot);
      slot = (slot == 2) ? 0 : slot + 1;
    }
    return;
  }
  const int wm = wave >> 1, wn = wave & 1, fr = lane & 15, q = lane >> 4;
  u32 boff[2];
  {
    const u32 row = (lane & 31) >> 2, f = row >> 1, base = 32768 + (lane >> 5) * 1024 + row * 128 + (lane & 3) * 8;
#pragma unroll
    for (int j = 0; j < 2; ++j) boff[j] = base + (((2 * wn + j) ^ f) << 5);
  }
  const u32 aoff = wm * 16384 + lane * 16;
  const int n0 = tn * 64 + wn * 32 + 4 * q;
  f32x4 acc[4][2];
  f32x4 gb[2];
  u32x2 xr[4][2];
#pragma unroll
  for (int j = 0; j < 2; ++j) {
    const u32x2 bv = *(const u32x2*)(bias + n0 + 16 * j);
    const f32x4 bf = {bf_lo(bv[0]), bf_hi(bv[0]), bf_lo(bv[1]), bf_hi(bv[1])};
    gb[j] = load_mod4(part, 5120 + n0 + 16 * j);
#pragma unroll
    for (int i = 0; i < 4; ++i) {
      acc[i][j] = bf;
      xr[i][j] = *(const u32x2*)(x1 + (size_t)(tm * 128 + wm * 64 + 16 * i + fr) * D_ + n0 + 16 * j);
    }
  }
  u32x4 fa[2][4], fb[2][2];
  auto ldfrag = [&](int buf, u32 st, int s) {
#pragma unroll
    for (int i = 0; i < 4; ++i) fa[buf][i] = *(LAS u32x4*)(lds + st + aoff + i * 4096 + s * 1024);
#pragma unroll
    for (int j = 0; j < 2; ++j) {
      const u32x2 lo = tr_read(lds + st + boff[j] + (2 * s) * 2048), hi = tr_read(lds + st + boff[j] + (2 * s + 1) * 2048);
      fb[buf][j] = (u32x4){lo[0], lo[1], hi[0], hi[1]};
    }
  };
  auto mma = [&](int buf) {
#pragma unroll
    for (int j = 0; j < 2; ++j)
#pragma unroll
      for (int i = 0; i < 4; ++i) acc[i][j] = mfma16(fb[buf][j], fa[buf][i], acc[i][j]);
  };
  auto sched = [&]() {
#pragma unroll
    for (int i = 0; i < 4; ++i) { __builtin_amdgcn_sched_group_barrier(0x8, 1, 0); __builtin_amdgcn_sched_group_barrier(0x100, 2, 0); }
    __builtin_amdgcn_sched_group_barrier(0x8, 4, 0);
    __builtin_amdgcn_sched_barrier(0);
  };
  RAW_BARRIER();
  ldfrag(0, 0, 0);
  u32 st = 0;
  for (int t = 0; t < G2_NT; ++t) {
    ldfrag(1, st, 1); mma(0); sched();
    ldfrag(0, st, 2); mma(1); sched();
    ldfrag(1, st, 3); mma(0); sched();
    LDS_BARRIER();
    st = (st == 2 * G2_STAGE) ? 0 : st + G2_STAGE;
    ldfrag(0, st, 0); mma(1); sched();
  }
#pragma unroll
  for (int i = 0; i < 4; ++i) {
    u32x2 o[2];
#pragma unroll
    for (int j = 0; j < 2; ++j) {
      const f32x4 xv = {bf_lo(xr[i][j][0]), bf_hi(xr[i][j][0]), bf_lo(xr[i][j][1]), bf_hi(xr[i][j][1])};
      const f32x4 r = xv + gb[j] * acc[i][j];
      o[j] = (u32x2){pk_bf16(r[0], r[1]), pk_bf16(r[2], r[3])};
    }
    auto s0 = __builtin_amdgcn_permlane16_swap(o[0][0], o[1][0], false, false);
    auto s1 = __builtin_amdgcn_permlane16_swap(o[0][1], o[1][1], false, false);
    const int m = tm * 128 + wm * 64 + 16 * i + fr;
    const int srow = (m >> 3) + 256 * (m & 7);
    *(u32x4*)(out + (size_t)srow * D_ + tn * 64 + wn * 32 + 16 * (q & 1) + 8 * (q >> 1)) = (u32x4){(u32)s0[0], (u32)s1[0], (u32)s0[1], (u32)s1[1]};
  }
}

extern "C" void kernel_launch(void* const* d_in, const int* in_sizes, int n_in, void* d_out, int out_size, void* d_ws, size_t ws_size,
                              hipStream_t stream) {
  const u16* x = (const u16*)d_in[0];
  const u16* wr = (const u16*)d_in[1];
  const u16* wi = (const u16*)d_in[2];
  const u16* w1 = (const u16*)d_in[4];
  const u16* b1 = (const u16*)d_in[5];
  const u16* w2 = (const u16*)d_in[6];
  const u16* b2 = (const u16*)d_in[7];
  const u16* c = (const u16*)d_in[8];
  const u16* wm = (const u16*)d_in[9];
  char* ws = (char*)d_ws;
  float* part = (float*)(ws + WS_MOD);
  u32* P1 = (u32*)(ws + WS_P1);
  u32* P2 = (u32*)(ws + WS_P2);
  u32* P2X = (u32*)(ws + WS_P2X);
  u16* y2 = (u16*)(ws + WS_Y2);
  u16* x1 = (u16*)(ws + WS_X1);
  u16* H = (u16*)(ws + WS_H);
  (void)hipFuncSetAttribute((const void*)k_gemm1, hipFuncAttributeMaxDynamicSharedMemorySize, G1_STAGE * G1_NS);
  (void)hipFuncSetAttribute((const void*)k_gemm2, hipFuncAttributeMaxDynamicSharedMemorySize, G2_STAGE * G2_NS);
  k_mod<<<768, 256, 0, stream>>>(c, wm, part);
  k_fft1<<<256, 256, 0, stream>>>(x, part, P1);
  k_fft2<<<257, 256, 0, stream>>>(P1, wr, wi, P2, P2X);
  k_fft3<<<256, 256, 0, stream>>>(P2, P2X, x, part, x1, y2);
  k_gemm1<<<256, 512, G1_STAGE * G1_NS, stream>>>(y2, w1, b1, H);
  k_gemm2<<<256, 512, G2_STAGE * G2_NS, stream>>>(H, w2, b2, part, x1, (u16*)d_out);
}
```

```cpp
#include <hip/hip_runtime.h>
#include <hip/hip_bf16.h>

typedef unsigned short u16;
typedef unsigned int u32;
typedef float f32x2 __attribute__((ext_vector_type(2)));
typedef float f32x4 __attribute__((ext_vector_type(4)));
typedef __bf16 bf16x2 __attribute__((ext_vector_type(2)));
typedef __bf16 bf16x8 __attribute__((ext_vector_type(8)));
typedef short s16x4 __attribute__((ext_vector_type(4)));
typedef u32 u32x2 __attribute__((ext_vector_type(2)));
typedef u32 u32x4 __attribute__((ext_vector_type(4)));
typedef __amdgpu_buffer_rsrc_t rsrc_t;
#define LAS __attribute__((address_space(3)))
#define GAS __attribute__((address_space(1)))
#define DI __device__ __forceinline__

constexpr int S_ = 2048, D_ = 1024, F_ = 3584, DF_ = 513;
constexpr float GELU_A = 0.4686686930532063f, GELU_B = 4.91222953926424f, GELU_IA = 2.1337034344781243f;

constexpr u32 WS_PART = 0;
constexpr u32 WS_EPOCH = 108 << 10;
constexpr u32 WS_CNT = 128 << 10;
constexpr u32 WS_P1 = 1 << 20;
constexpr u32 WS_Y2 = 10 << 20;
constexpr u32 WS_WT = 14 << 20;
constexpr u32 WS_XCH = 19 << 20;
constexpr u32 WS_H = 23 << 20;
constexpr u32 WS_END = 38 << 20;
constexpr u32 SENT = 0xffffffffu;
constexpr int SPIN_MAX = 1 << 18;

constexpr int X_DELAY = 4, G_DELAY = 6, XD1 = 24, XD2 = 4, PF_SPLIT = 5;

DI u32 pk_bf16(float lo, float hi) {
  f32x2 v = {lo, hi};
  return __builtin_bit_cast(u32, __builtin_convertvector(v, bf16x2));
}
DI float bf_lo(u32 w) { return __builtin_bit_cast(float, w << 16); }
DI float bf_hi(u32 w) { return __builtin_bit_cast(float, w & 0xffff0000u); }
DI f32x2 bf_2(u32 w) { return (f32x2){bf_lo(w), bf_hi(w)}; }
DI f32x2 round_bf2(f32x2 v) { return bf_2(pk_bf16(v.x, v.y)); }
DI float sin_rev(float r) { return __builtin_amdgcn_sinf(r); }
DI float cos_rev(float r) { return __builtin_amdgcn_cosf(r); }
DI f32x4 mfma16(u32x4 a, u32x4 b, f32x4 c) {
  return __builtin_amdgcn_mfma_f32_16x16x32_bf16(__builtin_bit_cast(bf16x8, a), __builtin_bit_cast(bf16x8, b), c, 0, 0, 0);
}
template <int CTRL> DI float dpp_mov(float x) {
  return __builtin_bit_cast(float, __builtin_amdgcn_update_dpp(0, __builtin_bit_cast(int, x), CTRL, 0xf, 0xf, true));
}
DI float swap32_add(float a, float b) {
  auto r = __builtin_amdgcn_permlane32_swap(__builtin_bit_cast(u32, a), __builtin_bit_cast(u32, b), false, false);
  return __builtin_bit_cast(float, (u32)r[0]) + __builtin_bit_cast(float, (u32)r[1]);
}
DI float swap16_add(float a, float b) {
  auto r = __builtin_amdgcn_permlane16_swap(__builtin_bit_cast(u32, a), __builtin_bit_cast(u32, b), false, false);
  return __builtin_bit_cast(float, (u32)r[0]) + __builtin_bit_cast(float, (u32)r[1]);
}
DI float row_allsum(float v) {
  v += dpp_mov<0x128>(v); v += dpp_mov<0x124>(v); v += dpp_mov<0x122>(v); v += dpp_mov<0x121>(v);
  return v;
}
DI float rdlane(float v, int l) { return __builtin_bit_cast(float, __builtin_amdgcn_readlane(__builtin_bit_cast(int, v), l)); }
#define PIN(x) asm volatile("" : "+v"(x))
#define SB() __builtin_amdgcn_sched_barrier(0)
#define RAW_BARRIER() do { asm volatile("" ::: "memory"); __builtin_amdgcn_s_barrier(); asm volatile("" ::: "memory"); } while (0)
#define LDS_BARRIER() do { asm volatile("s_waitcnt lgkmcnt(0)" ::: "memory"); __builtin_amdgcn_s_barrier(); asm volatile("" ::: "memory"); } while (0)
template <int N> DI void wait_vm() { asm volatile("s_waitcnt vmcnt(%0)" ::"n"(N) : "memory"); }
DI rsrc_t mk_rsrc(const void* p, u32 bytes) { return __builtin_amdgcn_make_buffer_rsrc((void*)p, 0, bytes, 0x00020000); }
DI void dma16(rsrc_t rs, LAS char* dst, u32 voff, u32 soff) { __builtin_amdgcn_raw_ptr_buffer_load_lds(rs, (LAS void*)dst, 16, voff, soff, 0, 0); }
template <int AUX> DI void st16(rsrc_t rs, u32x4 v, u32 voff, u32 soff) {
  __builtin_amdgcn_raw_buffer_store_b128(v, rs, voff, soff, AUX);
  asm volatile("s_nop 1" ::"v"(v));
}
template <int AUX> DI u32x4 ld16(rsrc_t rs, u32 voff, u32 soff) { return __builtin_amdgcn_raw_buffer_load_b128(rs, voff, soff, AUX); }
DI u32x2 tr_read(u32 addr) { return __builtin_bit_cast(u32x2, __builtin_amdgcn_ds_read_tr16_b64_v4i16((LAS s16x4*)(size_t)addr)); }
DI u32x4 lds_read16(u32 addr) { return *(LAS u32x4*)(size_t)addr; }

template <int AUX> DI void dma16x(rsrc_t rs, LAS char* dst, u32 voff, u32 soff) { __builtin_amdgcn_raw_ptr_buffer_load_lds(rs, (LAS void*)dst, 16, voff, soff, 0, AUX); }
DI void dma4(rsrc_t rs, LAS char* dst, u32 voff, u32 soff) { __builtin_amdgcn_raw_ptr_buffer_load_lds(rs, (LAS void*)dst, 4, voff, soff, 0, 0); }
DI u32 umax4(u32x4 v) { return max(max(v[0], v[1]), max(v[2], v[3])); }
DI u32 umin4(u32x4 v) { return min(min(v[0], v[1]), min(v[2], v[3])); }
DI u32x4 no_sent(u32x4 v) { return (u32x4){min(v[0], SENT - 1), min(v[1], SENT - 1), min(v[2], SENT - 1), min(v[3], SENT - 1)}; }
DI f32x4 round_bf4(f32x4 s) { const f32x2 a = round_bf2(s.xy), b = round_bf2(s.zw); return (f32x4){a.x, a.y, b.x, b.y}; }
DI f32x4 sum_mod4(const u32x4 (&p)[4]) {
  return round_bf4((__builtin_bit_cast(f32x4, p[0]) + __builtin_bit_cast(f32x4, p[1])) + (__builtin_bit_cast(f32x4, p[2]) + __builtin_bit_cast(f32x4, p[3])));
}
DI f32x4 bf_4(u32 a, u32 b) { return (f32x4){bf_lo(a), bf_hi(a), bf_lo(b), bf_hi(b)}; }

DI int pair_index(int u, int kb) { return kb ? 31 * u + kb - 1 : 124 + u; }
DI int chunk_pos0(int c) {
  const int m = -(c >> 7), u = ((c >> 5) ^ m) & 3, kb = ((c ^ m) - m) & 31;
  const int e = pair_index(u, kb);
  return (e >> 1) * 4 + ((e & 1) ^ (m & 3));
}
DI int wt_pos(int k1, int kd) {
  const int u = kd >> 7, kb = kd & 31, z = (kd >> 5) & 3;
  const int a = (u * 32 + kb) * 4 + z, b = 8 * pair_index(u, kb) + 2 * z + (k1 >> 2);
  const int m = (k1 & 3) ? -1 : 0;
  return ((a & m) | (b & ~m)) & 1023;
}

struct GemvRegs { u32x4 cv, w[8]; };
DI void gemv_issue(GemvRegs& g, const u16* __restrict__ c, const u16* __restrict__ wm, int row0, int col0, int lane) {
  const rsrc_t rm = mk_rsrc(wm, 1024 * 6144 * 2);
  g.cv = ld16<0>(mk_rsrc(c, 2048), (lane >> 3) * 16, row0 * 2);
#pragma unroll
  for (int p = 0; p < 8; ++p) g.w[p] = ld16<0>(rm, (lane >> 3) * (8 * 12288) + (lane & 7) * 16, (row0 + p) * 12288 + col0 * 2);
}
DI f32x2 gemv_sum(const GemvRegs& g) {
  float sc[8];
#pragma unroll
  for (int p = 0; p < 8; ++p) {
    float v = (p & 1) ? bf_hi(g.cv[p >> 1]) : bf_lo(g.cv[p >> 1]);
    v = v * __builtin_amdgcn_rcpf(1.0f + __builtin_amdgcn_exp2f(-1.44269504f * v));
    sc[p] = bf_lo(pk_bf16(v, v));
    PIN(sc[p]);
  }
  SB();
  f32x2 acc[4] = {};
#pragma unroll
  for (int p = 0; p < 8; ++p)
#pragma unroll
    for (int i = 0; i < 4; ++i) acc[i] += sc[p] * bf_2(g.w[p][i]);
  const float h0 = swap32_add(acc[0].x, acc[2].x), h1 = swap32_add(acc[0].y, acc[2].y), h2 = swap32_add(acc[1].x, acc[3].x), h3 = swap32_add(acc[1].y, acc[3].y);
  float k0 = swap16_add(h0, h2), k1 = swap16_add(h1, h3);
  k0 += dpp_mov<0x128>(k0); k1 += dpp_mov<0x128>(k1);
  return (f32x2){k0, k1};
}

template <bool SWAP> DI u32x4 cmul_pack(f32x4 re, f32x4 im, f32x4 c, f32x4 s) {
  const f32x2 r0 = re.xy * c.xy - im.xy * s.xy, i0 = re.xy * s.xy + im.xy * c.xy;
  const f32x2 r1 = re.zw * c.zw - im.zw * s.zw, i1 = re.zw * s.zw + im.zw * c.zw;
  if (SWAP) return (u32x4){pk_bf16(i0.x, r0.x), pk_bf16(i0.y, r0.y), pk_bf16(i1.x, r1.x), pk_bf16(i1.y, r1.y)};
  return (u32x4){pk_bf16(r0.x, i0.x), pk_bf16(r0.y, i0.y), pk_bf16(r1.x, i1.x), pk_bf16(r1.y, i1.y)};
}
DI u32x4 pack4(f32x4 re, f32x4 im) { return (u32x4){pk_bf16(re[0], im[0]), pk_bf16(re[1], im[1]), pk_bf16(re[2], im[2]), pk_bf16(re[3], im[3])}; }
DI u32 swap_lane0_32(u32 v, int t) {
  auto r = __builtin_amdgcn_permlane32_swap(v, v, false, false);
  return t == 0 ? (u32)r[1] : (t == 32 ? (u32)r[0] : v);
}
DI void roots4(int x, int q, int a, int b, int c, int d, float off, f32x4& cs, f32x4& sn) {
  const int u = a * x + b, v0 = c * 4 * q + d;
#pragma unroll
  for (int i = 0; i < 4; ++i) {
    const float r = (float)((u * (v0 + c * i)) & 2047) * (1.0f / 2048.0f) + off;
    cs[i] = cos_rev(r); sn[i] = -sin_rev(r);
  }
}
DI void wave_ln2(const f32x2 (&v)[2][8], float (&rs)[2], float (&nm)[2]) {
  float s[2], qq[2];
#pragma unroll
  for (int r = 0; r < 2; ++r) {
    f32x2 a = v[r][0], z = v[r][0] * v[r][0];
#pragma unroll
    for (int i = 1; i < 8; ++i) { a += v[r][i]; z += v[r][i] * v[r][i]; }
    s[r] = a.x + a.y; qq[r] = z.x + z.y;
  }
  const float u = row_allsum(swap16_add(swap32_add(s[0], qq[0]), swap32_add(s[1], qq[1])));
  auto sw = __builtin_amdgcn_permlane32_swap(__builtin_bit_cast(u32, u), __builtin_bit_cast(u32, u), false, false);
  const float mean = __builtin_bit_cast(float, (u32)sw[0]) * (1.0f / 1024.0f);
  const float var = fmaxf(__builtin_bit_cast(float, (u32)sw[1]) * (1.0f / 1024.0f) - mean * mean, 0.0f);
  const float r_ = __builtin_amdgcn_rsqf(var + 1e-5f), m_ = -mean * r_;
  rs[0] = rdlane(r_, 0); rs[1] = rdlane(r_, 16); nm[0] = rdlane(m_, 0); nm[1] = rdlane(m_, 16);
}
#define RETRY4(BAD, LOAD) do { \
    bool any_ = false; \
    _Pragma("unroll") for (int i = 0; i < 4; ++i) any_ |= (BAD); \
    if (__builtin_expect(__any(any_), 0)) { \
      for (int sp_ = 0; sp_ < SPIN_MAX; ++sp_) { \
        bool again_ = false; \
        asm volatile("" ::: "memory"); \
        _Pragma("unroll") for (int i = 0; i < 4; ++i) if (__any(BAD)) { LOAD; again_ = true; } \
        if (!again_) break; \
      } \
      wait_vm<0>(); \
    } } while (0)

constexpr int G1_BOFF = 49152, G1_BST = 4 * 7168, G1_NT = 16;
constexpr int G2_STAGE = 16384, G2_BOFF = 65536, G2_X1 = G1_BOFF + 3 * G1_BST, G2_HH = G2_X1 + 16384, G2_SYNC = G2_HH + 8192;
constexpr int L_GB = G2_SYNC + 256, L_B1 = L_GB + 512, L_JUNK = L_B1 + 1024, MAIN_LDS = L_JUNK + 256;
constexpr int G2_NM = 8, G2_NF = 24, G2_NT = 32;
constexpr int L_IMG = 0, L_DAT = 4096, L_PL2 = 8192, L_X = 12288, L_W = 16384, L_TW = 20480, L_MAT = 24576, L_ADR = L_MAT + 8 * 512;
constexpr int L_MOD = G2_X1 / 4, L_RED = L_MOD + 3072;

struct Fft2Consts { u32x4 mt[3][2]; f32x4 tw[4][2]; };
DI f32x4 lds_readf(u32 addr) { return *(LAS f32x4*)(size_t)addr; }
DI void fft2_consts(Fft2Consts& k, u32 cb) {
#pragma unroll
  for (int i = 0; i < 3; ++i)
#pragma unroll
    for (int h = 0; h < 2; ++h) k.mt[i][h] = lds_read16(cb + (L_MAT - L_TW + (4 + i) * 512 + h * 256) * 4);
#pragma unroll
  for (int i = 0; i < 4; ++i)
#pragma unroll
    for (int h = 0; h < 2; ++h) k.tw[i][h] = lds_readf(cb + ((4 + i) * 512 + h * 256) * 4);
}
template <int NC, bool PACKED, class GetW> DI void fft2_tail(f32x4 (&re)[NC], f32x4 (&im)[NC], const Fft2Consts& k, GetW getw) {
  const f32x4 z = {};
  u32x4 d[NC];
  SB();
#pragma unroll
  for (int c = 0; c < NC; ++c) { const int t0 = (PACKED && !(c & 1)) ? 2 : 0; d[c] = cmul_pack<false>(re[c], im[c], k.tw[t0][0], k.tw[t0][1]); }
  SB();
#pragma unroll
  for (int c = 0; c < NC; ++c) { re[c] = mfma16(k.mt[0][0], d[c], z); im[c] = mfma16(k.mt[0][1], d[c], z); }
  u32x4 wp[NC];
  getw(wp);
  SB();
#pragma unroll
  for (int c = 0; c < NC; ++c)
    d[c] = cmul_pack<true>(re[c], im[c], (f32x4){bf_lo(wp[c][0]), bf_lo(wp[c][1]), bf_lo(wp[c][2]), bf_lo(wp[c][3])},
                           (f32x4){bf_hi(wp[c][0]), bf_hi(wp[c][1]), bf_hi(wp[c][2]), bf_hi(wp[c][3])});
  SB();
#pragma unroll
  for (int c = 0; c < NC; ++c) { re[c] = mfma16(d[c], k.mt[0][0], z); im[c] = mfma16(d[c], k.mt[0][1], z); }
  SB();
#pragma unroll
  for (int c = 0; c < NC; ++c) { const int t1 = (PACKED && !(c & 1)) ? 3 : 1; d[c] = cmul_pack<false>(re[c], im[c], k.tw[t1][0], k.tw[t1][1]); }
  SB();
#pragma unroll
  for (int c = 0; c < NC; ++c) { const int m = (PACKED && !(c & 1)) ? 0 : 2; re[c] = mfma16(k.mt[m][0], d[c], z); im[c] = mfma16(k.mt[m][1], d[c], z); }
  SB();
}
template <int NJ, class GetW> DI void fft2_packed(LAS u32x4* const (&pa)[NJ], LAS u32x4* const (&pm)[NJ], const Fft2Consts& k, GetW getw) {
  const f32x4 z = {};
  f32x4 re[2 * NJ], im[2 * NJ];
  u32x4 da[NJ], dm[NJ];
#pragma unroll
  for (int j = 0; j < NJ; ++j) { da[j] = *pa[j]; dm[j] = *pm[j]; }
#pragma unroll
  for (int j = 0; j < NJ; ++j) {
    const u32x4 db = dm[j] ^ 0x80000000u, dn = dm[j] ^ 0x00008000u;
    re[2 * j] = mfma16(db, k.mt[0][0], mfma16(da[j], k.mt[0][0], z)); im[2 * j] = mfma16(db, k.mt[0][1], mfma16(da[j], k.mt[0][1], z));
    re[2 * j + 1] = mfma16(dn, k.mt[1][0], mfma16(da[j], k.mt[1][0], z)); im[2 * j + 1] = mfma16(dn, k.mt[1][1], mfma16(da[j], k.mt[1][1], z));
  }
  fft2_tail<2 * NJ, true>(re, im, k, getw);
#pragma unroll
  for (int j = 0; j < NJ; ++j) {
    *pm[j] = pack4(im[2 * j + 1] - re[2 * j], im[2 * j] + re[2 * j + 1]);
    *pa[j] = pack4(re[2 * j] + im[2 * j + 1], im[2 * j] - re[2 * j + 1]);
  }
}

#define MFMA_IP(c, x, y) do { asm volatile("v_mfma_f32_16x16x32_bf16 %0, %1, %2, %0" : "+v"(c) : "v"(x), "v"(y)); SB(); } while (0)
#define MM(i, j, f) MFMA_IP(acc[i][j], fb[j][f], fa[i][f])
#define UPD(x, d) do { asm volatile("v_add_u32 %0, %1, %0" : "+v"(x) : "s"(d)); SB(); } while (0)
#define UP(x) UPD(x, delta)
#define PP(i, j, f) do { MM(i, j, f); MM(i, j, (f) ^ 1); } while (0)
#define PU(i, j, f, x, y) do { MM(i, j, f); UP(x); MM(i, j, (f) ^ 1); UP(y); } while (0)
#define PA(i, j, f, ia) do { MM(i, j, f); rdA(ia, 0); MM(i, j, (f) ^ 1); rdA(ia, 1); } while (0)
#define PB(i, j, f, jb, s) do { MM(i, j, f); rdB(jb, jb, s, 0); MM(i, j, (f) ^ 1); rdB(jb, jb, s, 1); } while (0)
#define MFMA_DRAIN() do { asm volatile("s_nop 7\n\ts_nop 1"); SB(); } while (0)
DI void gelu16(const f32x4& a0, const f32x4& a1, const f32x4& a2, const f32x4& a3, u32 (&o)[8]) {
  const f32x2 y[8] = {a0.xy, a0.zw, a1.xy, a1.zw, a2.xy, a2.zw, a3.xy, a3.zw};
  f32x2 t[8];
#pragma unroll
  for (int i = 0; i < 8; ++i) t[i] = y[i] * y[i] + GELU_B;
  SB();
#pragma unroll
  for (int i = 0; i < 8; ++i) t[i] = -t[i] * y[i];
  SB();
#pragma unroll
  for (int i = 0; i < 8; ++i) t[i] = (f32x2){__builtin_amdgcn_exp2f(t[i].x), __builtin_amdgcn_exp2f(t[i].y)};
  SB();
#pragma unroll
  for (int i = 0; i < 8; ++i) t[i] = t[i] + 1.0f;
  SB();
#pragma unroll
  for (int i = 0; i < 8; ++i) t[i] = (f32x2){__builtin_amdgcn_rcpf(t[i].x), __builtin_amdgcn_rcpf(t[i].y)};
  SB();
#pragma unroll
  for (int i = 0; i < 8; ++i) t[i] = t[i] * y[i];
  SB();
#pragma unroll
  for (int i = 0; i < 8; ++i) o[i] = pk_bf16(t[i].x, t[i].y);
}

struct Args { const u16 *x, *wr, *wi, *cvec, *wm, *w1, *b1, *w2, *b2; char* ws; u16* out; };
__global__ __launch_bounds__(512, 2) void k_main(Args) {
  extern __shared__ __attribute__((aligned(16))) char smem[];
  LAS char* lds = (LAS char*)smem;
  LAS u32* ldw = (LAS u32*)smem;
  LAS float* ldf = (LAS float*)smem;
  const __attribute__((address_space(4))) Args* ka = (const __attribute__((address_space(4))) Args*)__builtin_amdgcn_kernarg_segment_ptr();
  unsigned long long pc0;
  {
    unsigned long long ex;
    u32 off = (threadIdx.x * 8 + ((blockIdx.x >> 3) & 7)) << 7;
    asm volatile("s_getpc_b64 %0\n.Lk_main_pc:\n\tv_cmp_gt_u32 vcc, .Lk_main_end-.Lk_main_pc, %3\n\ts_mov_b32 m0, %4\n\ts_and_saveexec_b64 %1, vcc\n\t"
                 "s_cbranch_execz .Lk_main_nopf\n\tglobal_load_lds_dword %3, %0\n.Lk_main_nopf:\n\ts_mov_b64 exec, %1"
                 : "=&s"(pc0), "=&s"(ex), "+s"(ka) : "v"(off), "s"((u32)(size_t)lds + L_JUNK) : "vcc", "scc", "memory");
  }
  const u16 *x = ka->x, *wr = ka->wr, *wi = ka->wi, *cvec = ka->cvec, *wm = ka->wm, *w1 = ka->w1, *b1 = ka->b1, *w2 = ka->w2, *b2 = ka->b2;
  char* ws = ka->ws;
  u16* out = ka->out;
  if (threadIdx.x < 32) ((LAS u32*)(lds + G2_SYNC))[threadIdx.x] = 0u;
  asm volatile("" ::"s"(x), "s"(wr), "s"(wi), "s"(cvec), "s"(wm), "s"(w1), "s"(b1), "s"(w2), "s"(b2), "s"(ws), "s"(out));
  const int b = blockIdx.x, tid = threadIdx.x, lane = tid & 63, wave = __builtin_amdgcn_readfirstlane(tid >> 6);
  const int tm = 8 * ((b & 7) >> 2) + ((b >> 3) & 7), tn = 4 * (b & 3) + (b >> 6), kh = tn >> 3, tn2 = tn & 7, s2 = tm * 16 + tn;
  const int partner = b ^ 2, hidx = (b >> 2) * 2 + (b & 1);
  const int col0 = 224 * tn, odd = tn & 1;
  const int gcol = col0 + (odd ? 32 : 0), hcol = col0 + (odd ? 0 : 192);
  const int cls2 = b & 3, tile = b >> 2, tileid = cls2 * 64 + tile;
  const int xl = lane & 15, q = lane >> 4;
  const u32 l16 = lane * 16;
  const rsrc_t rw = mk_rsrc(ws, WS_END), ro = mk_rsrc(out, S_ * D_ * 2);
  volatile LAS u32* lsync = (volatile LAS u32*)(lds + G2_SYNC);
  const f32x4 z = {};
  u32 tag;
  auto get_tag = [&]() { tag = __builtin_amdgcn_readfirstlane(lsync[22]); };
  auto lds_wait = [&](int idx, u32 want) {
#pragma unroll 1
    for (int sp = 0; sp < 64 * SPIN_MAX && lsync[idx] < want; ++sp) __builtin_amdgcn_s_sleep(2);
    asm volatile("" ::: "memory");
  };
  auto lds_wait4 = [&](int idx, u32 want) {
#pragma unroll 1
    for (int sp = 0; sp < 64 * SPIN_MAX && !__all(lsync[idx + (lane & 3)] >= want); ++sp) __builtin_amdgcn_s_sleep(1);
    asm volatile("" ::: "memory");
  };
  auto arrive = [&](int idx, u32 n) {
    u32 last = 0;
    if (lane == 0) asm volatile("ds_add_rtn_u32 %0, %1, %2\n\ts_waitcnt lgkmcnt(0)" : "=&v"(last) : "v"((u32)(size_t)(lsync + idx)), "v"(1u) : "memory");
    return __builtin_amdgcn_readfirstlane(last) == n - 1;
  };
  const u32x4 sv = {SENT, SENT, SENT, SENT};
  auto mark_p1 = [&]() {
#pragma unroll
    for (int i = 0; i < 8; ++i) st16<16>(rw, sv, l16, WS_P1 + b * 16384 + (wave - 4) * 8192 + i * 1024);
    SB();
  };
  if (wave >= 6) {
    __builtin_amdgcn_s_setprio(3);
    const int cg = 2 * ((b & 7) + 8 * ((b >> 4) & 3)) + ((b >> 3) & 1), row0 = (b >> 6) * 256 + (wave - 6) * 128;
    GemvRegs gr;
    const u32 ep = __builtin_amdgcn_raw_buffer_load_b32(rw, 0, WS_EPOCH, 16);
    gr.cv = ld16<0>(mk_rsrc(cvec, 2048), (lane >> 2) * 16, row0 * 2);
#pragma unroll
    for (int i = 0; i < 8; ++i) gr.w[i] = ld16<0>(mk_rsrc(wm, 1024 * 6144 * 2), (lane >> 2) * (8 * 12288) + (lane & 3) * 16, (row0 + i) * 12288 + cg * 64);
    SB();
    LDS_BARRIER();
    if (wave == 6) lsync[22] = ep + 0x01010101u;
    f32x2 ks = gemv_sum(gr);
    ks.x += dpp_mov<0x124>(ks.x); ks.y += dpp_mov<0x124>(ks.y);
    *(LAS f32x2*)(ldf + L_RED + (wave - 6) * 32 + (lane & 3) * 8 + 2 * q) = ks;
    wait_vm<0>();
    u32 last = 0, marks = 0;
    f32x4 r0 = {}, r1 = {};
    if (lane < 8) asm volatile("ds_add_rtn_u32 %0, %4, %5 offset:4\n\tds_read_b32 %1, %4 offset:64\n\tds_read_b128 %2, %6\n\tds_read_b128 %3, %6 offset:128\n\ts_waitcnt lgkmcnt(0)"
                 : "=&v"(last), "=&v"(marks), "=&v"(r0), "=&v"(r1)
                 : "v"((u32)(size_t)lsync), "v"(lane == 0 ? 1u : 0u), "v"((u32)(size_t)(ldf + L_RED) + (lane & 7) * 16) : "memory");
    if (__builtin_amdgcn_readfirstlane(last) == 1u) {
      if (__builtin_amdgcn_readfirstlane(marks) < 2u) lds_wait(16, 2);
      const u32 tg = ep + 0x01010101u;
      const u32x4 o = (__builtin_bit_cast(u32x4, r0 + r1) & 0xffffff00u) | ((u32x4){tg, tg >> 8, tg >> 16, tg >> 24} & 0xffu);
      if (lane < 8) st16<16>(rw, o, l16, WS_PART + (b >> 6) * 24576 + cg * 128);
      lsync[3] = 1u;
      wait_vm<0>();
      lsync[3] = 2u;
    }
    __builtin_amdgcn_s_setprio(0);
  } else {
    if (wave >= 4) mark_p1();
    LDS_BARRIER();
  }
  auto make_tw = [&]() {
    const int k1 = cls2 ? cls2 : 4;
    f32x4 cs, sn;
    const bool f = wave < 4, o = wave & 1;
    const int kk = (wave < 6) ? k1 : 0;
    const float amp = (!f && !o && !cls2) ? 0.5f : 1.0f;
    roots4(xl, q, f ? 2 : (o ? 1 : 8), f ? 32 * (wave >> 1) : (o ? 0 : kk), (!f && o) ? 8 : 1, f ? 16 * (wave & 1) : (o ? kk : 0),
           (wave == 4 && !cls2) ? 0.25f : 0.f, cs, sn);
    *(LAS f32x4*)(ldf + L_TW + wave * 512 + lane * 4) = cs * amp;
    *(LAS f32x4*)(ldf + L_TW + wave * 512 + 256 + lane * 4) = sn * amp;
  };
  auto make_mat = [&]() {
    const int k1 = cls2 ? cls2 : 4;
    f32x4 cs, sn;
    const bool f = wave < 4;
    roots4(xl, q, f ? 64 : (wave == 6 ? 16 : 128), f ? 1024 * (wave >> 1) : (wave == 5 ? 16 * k1 : 0), wave == 6 ? 8 : 1,
           f ? 16 * (wave & 1) : (wave == 6 ? k1 : 0), 0.f, cs, sn);
    *(LAS u32x4*)(ldw + L_MAT + wave * 512 + lane * 4) = pack4(cs, -sn);
    *(LAS u32x4*)(ldw + L_MAT + wave * 512 + 256 + lane * 4) = pack4(sn, cs);
  };
  u32 cb = (u32)(size_t)lds + L_TW * 4 + l16;
  PIN(cb);
  auto colp = [&](int e) { return (LAS u32x4*)(ldw + L_DAT) + e * 64 + (lane ^ ((e >> 2) << 1)); };

  if (wave >= 4) {
    const int p = wave - 4;
    auto consts = [&]() {
      const int c1 = (4 * (p & 1) + q) * 32 + 16 * (p >> 1) + xl, c0 = chunk_pos0(c1);
      ldw[L_ADR + lane * 4 + p] = (c0 >> 2) * 16384 + (c0 & 3) * 16;
      ldw[L_ADR + 256 + lane * 4 + p] = (c1 >> 2) * 16384 + (c1 & 3) * 16;
      make_tw(); make_mat();
      asm volatile("" ::: "memory");
      lsync[12 + p] = 1u;
    };
    if (p >= 2) {
      consts();
      lsync[12 + p] = p == 3 ? 9u : 3u;
      lds_wait(3, 1);
      get_tag();
    } else {
      consts();
      wait_vm<0>();
      arrive(16, 2);
      if (p == 1) {
        if ((lane & 31) == (b >> 3)) {
          const char* base = lane < 8 ? (const char*)w1 : lane < 16 ? (const char*)w2 : lane < 20 ? (const char*)out : lane < 22 ? (const char*)wr : lane < 24 ? (const char*)wi : ws;
          const u32 idx = lane < 16 ? min(lane & 7, 6) : lane < 20 ? lane - 16 : lane < 24 ? (lane & 1) : min(lane - 24, 37);
          __builtin_amdgcn_global_load_lds((const GAS char*)base + idx * (1u << 20) + (b & 31) * 128, (LAS void*)(lds + L_JUNK), 4, 0, 16);
        }
      } else if (lane < 6) {
        __builtin_amdgcn_raw_ptr_buffer_load_lds(rw, (LAS void*)(lds + L_JUNK), 4,
            lane < 4 ? WS_CNT + tm * 8192 + tn * 128 + lane * 2048 : WS_WT + (4 << 20) + (lane - 4) * 1024 + (s2 >> 2) * 16, 0, 0, 16);
      }
      const int cls = s2 & 3, rg = s2 >> 2, sec = p;
      const bool mir = sec && cls;
      const int k1 = (sec && !cls) ? 4 : cls, ks0 = k1 + 8 * (rg & 15) + 512 * (rg >> 4);
      const int col = lane * 8 + (k1 & 1);
      const rsrc_t rr = mk_rsrc(wr, S_ * DF_ * 2), ri = mk_rsrc(wi, S_ * DF_ * 2);
      LAS u32* dst = ldw + L_PL2;
      auto lpos = [&](int g) { g ^= rg & 15; return (g ^ ((g >> 4) & 6)) << 2; };
      lds_wait(1, 1);
      u32x4 re[4], im[4];
#pragma unroll
      for (int i = 0; i < 4; ++i) {
        const int row = mir ? 2048 - ks0 - 128 * i : ks0 + 128 * i;
        re[i] = ld16<0>(rr, col * 2, row * (DF_ * 2));
        im[i] = ld16<0>(ri, col * 2, row * (DF_ * 2));
      }
      const int si = lane & 3, skd = (lane & 4) * 128, sks = ks0 + 128 * si;
      const u32 sp = ((const GAS u16*)((lane & 16) ? wi : wr))[((lane & 8) ? (2048 - sks) & 2047 : sks) * DF_ + skd];
      SB();
      int pos[8];
#pragma unroll
      for (int e = 0; e < 8; ++e) { pos[e] = lpos(wt_pos(k1, (mir ? 1024 - col - e : col + e) & 1023)); PIN(pos[e]); }
      const int spos = lpos(wt_pos(k1, skd)) + si;
      SB();
      const u32 sg = mir ? 0x80000000u : 0u;
#pragma unroll
      for (int e = 0; e < 8; ++e) {
        u32x4 o;
#pragma unroll
        for (int i = 0; i < 4; ++i) o[i] = __builtin_amdgcn_perm(im[i][e >> 1], re[i][e >> 1], (e & 1) ? 0x07060302u : 0x05040100u) ^ sg;
        if ((e != 0 && e != 7) || ((col + e) & 511)) *(LAS u32x4*)(dst + pos[e]) = o;
      }
      {
        const float v = bf_lo(sp), vm = dpp_mov<0x128>(v);
        const float h = 0.5f * ((lane & 16) ? v - vm : v + vm);
        auto sw = __builtin_amdgcn_permlane16_swap(__builtin_bit_cast(u32, h), __builtin_bit_cast(u32, h), false, false);
        const u32 o = pk_bf16(h, __builtin_bit_cast(float, (u32)sw[1]));
        if (lane < 8 && !mir) {
          if (!cls && skd) __builtin_amdgcn_raw_buffer_store_b32(o, rw, ((k1 >> 2) * 64 + rg) * 16 + si * 4, WS_WT + (4 << 20), 16);
          else dst[spos] = o;
        }
      }
      asm volatile("" ::: "memory");
      lsync[28 + p] = 1u;
      lds_wait(28 + (p ^ 1), 1);
      lds_wait(8 + p, 1);
      u32x4 o[8];
#pragma unroll
      for (int i = 0; i < 8; ++i) { const int g = (sec * 8 + i) * 64 + lane; o[i] = *(LAS u32x4*)(dst + ((g ^ ((g >> 4) & 6)) << 2)); }
#pragma unroll
      for (int i = 0; i < 8; ++i) st16<16>(rw, o[i], q * 16384 + xl * 16, WS_WT + cls * (1 << 20) + (sec * 8 + i) * 65536 + rg * 256);
      arrive(19, 2);
      wait_vm<0>();
      lsync[12 + p] = 2u;
      get_tag();
    }
    const rsrc_t rb = mk_rsrc(w1, D_ * F_ * 2), rb2 = mk_rsrc(w2, F_ * D_ * 2);
    const u32x4 tb = (u32x4){tag, tag >> 8, tag >> 16, tag >> 24} & 0xffu;
    if (p >= 2) {
      lds_wait(19, 2);
      GemvRegs ga, gb_;
      gemv_issue(ga, cvec, wm, (b >> 6) * 256 + (p - 2) * 128, 2048 + (b & 63) * 64, lane);
      SB();
      wait_vm<0>();
      gemv_issue(gb_, cvec, wm, (b >> 6) * 256 + (p - 2) * 128 + 64, 2048 + (b & 63) * 64, lane);
      SB();
      f32x2 ks = gemv_sum(ga);
      PIN(ks);
      ks += gemv_sum(gb_);
      *(LAS f32x2*)(ldf + L_RED + 64 + (p - 2) * 64 + (lane & 7) * 8 + 2 * q) = ks;
      if (arrive(21, 2)) {
        LAS f32x4* red = (LAS f32x4*)(ldf + L_RED + 64) + xl;
        const u32x4 o = (__builtin_bit_cast(u32x4, red[0] + red[16]) & 0xffffff00u) | tb;
        if (lane < 16) st16<16>(rw, o, l16, WS_PART + (b >> 6) * 24576 + 8192 + (b & 63) * 256);
      }
    }
    if (p == 3) {
#pragma unroll
      for (int i = 0; i < 8; ++i) {
        if (i == 0) lds_wait(18, 3);
        if (i == PF_SPLIT) lds_wait4(8, 5);
        if (lane < 56) {
          if (i < 4) dma4(rb, lds + L_JUNK, (lane / 14) * (64 * F_ * 2) + (lane % 14) * 128, (i * 256 + (b >> 2)) * (F_ * 2) + (b & 3) * 1792);
          else dma4(rb2, lds + L_JUNK, ((i - 4) * 7 + (lane >> 3)) << 17 | (lane & 7) << 7, kh * (1792 * 2048) + hidx * 1024);
        }
      }
    } else {
      const u32x2 bv2 = *(const GAS u32x2*)(b2 + tn2 * 128 + kh * 64 + xl * 4);
      *(LAS u32x4*)(lds + L_B1 + l16) = ld16<0>(mk_rsrc(b1, F_ * 2), l16, col0 * 2);
      Fft2Consts k;
      const bool fold = (b == 62 * 4 && p == 1);
      if (fold) { lds_wait4(12, 2); fft2_consts(k, cb); }
      lds_wait4(24, 3);
      lsync[18] = 3u;
      if (fold) {
        const u32x4 wx0 = ld16<16>(rw, l16, WS_WT + (4 << 20)), wx1 = ld16<16>(rw, l16, WS_WT + (4 << 20) + 1024);
        LAS u32x4* const pa[1] = {colp(8)};
        fft2_packed<1>(pa, pa, k, [&](u32x4 (&wp)[2]) { wp[0] = wx0; wp[1] = wx1; });
        asm volatile("" ::: "memory");
      }
      lsync[12 + p] = 3u;
      lds_wait(8 + p, 3);
      u32x4 pg[4][4], pb;
      u32 bad;
      auto gather = [&]() {
#pragma unroll
        for (int c = 0; c < 4; ++c)
#pragma unroll
          for (int i = 0; i < 4; ++i) pg[c][i] = ld16<16>(rw, l16, WS_PART + i * 24576 + (2 + p) * 4096 + c * 1024);
        pb = ld16<16>(rw, q * 24576 + xl * 16, WS_PART + (5120 + tn2 * 128 + kh * 64) * 4);
        pb ^= tb;
        u32x4 o = pb;
#pragma unroll
        for (int c = 0; c < 4; ++c)
#pragma unroll
          for (int i = 0; i < 4; ++i) { pg[c][i] ^= tb; o |= pg[c][i]; }
        bad = ((o[0] | o[1]) | (o[2] | o[3])) & 0xffu;
      };
      int sp = 0;
      do { asm volatile("" ::: "memory"); gather(); } while (__any(bad != 0u) && ++sp < SPIN_MAX);
#pragma unroll
      for (int c = 0; c < 4; ++c) {
        const int pos = p == 0 ? 2048 + c * 256 + lane * 4 : (p - 1) * 1024 + ((c >> 1) * 2 + (lane & 1)) * 256 + ((c & 1) * 32 + (lane >> 1)) * 4;
        *(LAS f32x4*)(ldf + L_MOD + pos) = round_bf4(sum_mod4(pg[c]) + (p == 2 ? 1.0f : 0.0f)) * (p == 0 ? 1.0f / 2097152.0f : 1.0f);
      }
      asm volatile("" ::: "memory");
      lsync[12 + p] = 4u;
      const f32x4 pf = __builtin_bit_cast(f32x4, pb);
      f32x4 g;
#pragma unroll
      for (int e = 0; e < 4; ++e) { const float h = swap16_add(pf[e], pf[e]); g[e] = swap32_add(h, h); }
      g = round_bf4(g);
      LAS f32x4* gl = (LAS f32x4*)(lds + L_GB);
      gl[xl] = g * GELU_IA;
      gl[16 + xl] = g * bf_4(bv2[0], bv2[1]);
    }
    const u32 r8 = lane >> 3;
    const u32 swz = ((((lane & 7) >> 1) ^ (r8 >> 1)) << 5) | ((lane & 1) << 4);
    const u32 va0 = r8 * 2048 + (((lane & 7) ^ (r8 >> 1)) << 4), va1 = va0 ^ 64;
    const u32 vb = (8 * (r8 >> 2) + (r8 & 3)) * (F_ * 2) + swz;
    const u32 r16 = lane >> 2;
    const u32 vh = (8 * (r16 >> 2) + (r16 & 3)) * (F_ * 2) + (((((lane >> 1) & 1) ^ ((r16 >> 2) & 1)) << 5) | ((lane & 1) << 4));
    const u32 sa0 = WS_Y2 + (tm * 128 + p * 32) * 2048;
    const u32 sb0 = (32 * (p >> 1) + 4 * (p & 1)) * (F_ * 2);
    auto issue1A = [&](int t, int slot) {
#pragma unroll
      for (int i = 0; i < 4; ++i) dma16x<16>(rw, lds + slot * 16384 + (4 * p + i) * 1024, (i & 1) ? va1 : va0, sa0 + t * 128 + i * 8 * 2048);
      SB();
    };
    auto issue1B = [&](int t, int slot) {
      const u32 sb = sb0 + t * 64 * (F_ * 2);
      LAS char* sbp = lds + G1_BOFF + slot * G1_BST + p * 7168;
#pragma unroll
      for (int g = 0; g < 3; ++g)
#pragma unroll
        for (int hb = 0; hb < 2; ++hb) dma16(rb, sbp + g * 2048 + hb * 1024, vb, sb + hb * 16 * (F_ * 2) + (gcol + 64 * g) * 2);
      dma16(rb, sbp + 6144, vh, sb + hcol * 2);
      SB();
    };
    auto kblk = [&](int t, int s) {
      if (t >= G2_NF) return 14 * ((t - G2_NF + 4 * kh) & 7) + 6 + s;
      int u = tn2, g = t;
      if (t >= 3) { g = (t - 3) / 7; u = t - 3 - 7 * g; u += (u >= tn2); }
      const int tt = 8 * kh + u;
      return 7 * tt + (tt & 1) + (g == 2 ? 2 + s : g + 4 * s);
    };
    int kv0 = kblk(lane & 31, 0), kv1 = kblk(lane & 31, 1);
    PIN(kv0); PIN(kv1);
    const u32 sh0 = WS_H + (tm * 8 + 2 * p) * 112 * 1024;
    const u32 vb0 = r8 * 2048 + swz, vb1 = (8 * (r8 >> 2) + (r8 & 3)) * 2048 + swz;
    auto issueA = [&](int t) {
      LAS char* st = lds + (t & 3) * G2_STAGE + p * 4096;
      const u32 k0 = __builtin_amdgcn_readlane(kv0, t) * 1024, k1 = __builtin_amdgcn_readlane(kv1, t) * 1024;
#pragma unroll
      for (int i = 0; i < 4; ++i) dma16x<16>(rw, st + i * 1024, l16, sh0 + (i >> 1) * 112 * 1024 + ((i & 1) ? k1 : k0));
      SB();
    };
    auto issueB = [&](int t, bool nat) {
      LAS char* st = lds + G2_BOFF + (t & 3) * G2_STAGE + p * 4096;
      const u32 sb = (__builtin_amdgcn_readlane(p < 2 ? kv0 : kv1, t) * 32 + (nat ? 4 : 16) * (p & 1)) * 2048 + tn2 * 256;
#pragma unroll
      for (int g = 0; g < 2; ++g)
        if (g == 0 || !nat) {
#pragma unroll
          for (int hb = 0; hb < 2; ++hb) dma16(rb2, st + g * 2048 + hb * 1024, nat ? vb1 : vb0, sb + hb * (nat ? 16 : 8) * 2048 + (g ^ kh) * 128);
        }
      SB();
    };
    GAS u32* mbox = (GAS u32*)(ws + WS_CNT) + tm * 2048;
    GAS u32* mine0 = mbox + tn * 32 + 8 * kh + (lane & 7);
    GAS u32* mine1 = mbox + 512 + tn * 32 + (lane & 15);
    auto poll = [&](GAS u32* c) {
#pragma unroll 1
      for (int sp = 0; sp < SPIN_MAX; ++sp) {
        if (__all(__hip_atomic_load(c, __ATOMIC_RELAXED, __HIP_MEMORY_SCOPE_AGENT) == tag)) break;
        __builtin_amdgcn_s_sleep(2);
      }
      asm volatile("" ::: "memory");
    };
    auto signal = [&](int lvl) {
      if (lane < (lvl ? 16 : 8)) __hip_atomic_store(mbox + lvl * 512 + (lvl ? lane : 8 * kh + lane) * 32 + tn, tag, __ATOMIC_RELAXED, __HIP_MEMORY_SCOPE_AGENT);
    };
    lds_wait4(8, 5);
    issue1B(0, 0);
    lds_wait4(24, 6);
    issue1B(1, 1);
    __builtin_amdgcn_s_setprio(3);
    lds_wait(8 + p, 6);
    issue1B(2, 2);
    if (p == 0) { lds_wait4(24, 8); signal(2); poll(mine1 + 512); }
    RAW_BARRIER();
    issue1A(0, 0); issue1A(1, 1); issue1A(2, 2);
    wait_vm<8>();
    RAW_BARRIER();
    wait_vm<4>();
    RAW_BARRIER();
    issue1A(3, 0); issue1B(3, 0);
    int slot = 1;
#pragma unroll 1
    for (int t = 1; t < G1_NT - 3; ++t) {
      wait_vm<11>();
      RAW_BARRIER();
      issue1A(t + 3, slot); issue1B(t + 3, slot);
      slot = (slot == 2) ? 0 : slot + 1;
    }
    wait_vm<11>();
    RAW_BARRIER();
    {
#pragma unroll
      for (int i = 0; i < 4; ++i) st16<16>(rw, sv, l16, WS_XCH + (partner * 4 + p) * 4096 + i * 1024);
    }
    issueB(1, false);
    if (p == 3) {
      u32 len;
      asm volatile("s_mov_b32 %0, .Lk_main_end-.Lk_main_pc" : "=s"(len));
      if (lane < 16) dma4(mk_rsrc((const void*)pc0, len), lds + L_JUNK, (lane * 32 + (b >> 3)) * 128, 0);
      SB();
    }
    wait_vm<8>();
    RAW_BARRIER();
    issueB(2, false); issueB(3, false);
    RAW_BARRIER();
    issueB(0, false);
    {
      const u32 ooff = ((tm * 16 + (p >> 1) * 8 + (xl >> 3)) + 256 * (lane & 7)) * 2048 + (q & 1) * 32 + (q >> 1) * 16;
#pragma unroll
      for (int i = 0; i < 4; ++i) dma16x<16>(ro, lds + G2_X1 + (p * 4 + i) * 1024, ooff, i * 4096 + (tn2 * 128 + kh * 64 + (p & 1) * 32) * 2);
    }
    u32x4 hv[4];
    LAS char* hl = lds + (p >> 1) * 8192 + l16;
    const u32 hb = WS_H + ((tm * 8 + (p >> 1) * 4) * 112 + (gcol >> 5)) * 1024;
    const u32 vhh = (q & 1) * 112 * 1024 + ((q >> 1) * 16 + xl) * 16;
    auto moveH = [&](int n, int wn_) {
      if (n < 12) {
#pragma unroll 1
        for (int sp = 0; sp < 64 * SPIN_MAX && lsync[4 + (p & 2) + wn_] < n + 2u; ++sp) __builtin_amdgcn_s_sleep(2);
        asm volatile("" ::: "memory");
      }
#pragma unroll
      for (int k = 0; k < 2; ++k)
        hv[k] = (n < 12) ? *(LAS u32x4*)(hl + wn_ * 1024 + (n >> 2) * G2_STAGE + ((n & 3) + k) * 2048) : *(LAS u32x4*)(lds + G2_HH + (((p & 2) + wn_) * 2 + k) * 1024 + l16);
      if (n == 6) {
        wait_vm<2>();
        if (arrive(0, 4)) signal(0);
      }
      SB();
#pragma unroll
      for (int k = 0; k < 2; ++k) {
        if (n < 12) st16<16>(rw, hv[k], l16, hb + ((n & 3) + k) * 112 * 1024 + (n < 8 ? (n >> 2) + 4 * wn_ : 2 + wn_) * 1024);
        else st16<16>(rw, hv[k], vhh, hb + k * 224 * 1024 + (odd ? -1024 : 6144) + wn_ * 512);
      }
      SB();
    };
    u32 lv = ~tag, lv1 = ~tag;
    auto look = [&](u32& d, GAS u32* c) { asm volatile("global_load_dword %0, %1, off sc1" : "+v"(d) : "v"(c) : "memory"); };
#pragma unroll
    for (int n = 0; n < 8; n += 2) moveH(n, p & 1);
    if (p != 0) {
#pragma unroll
      for (int n = 8; n < 12; n += 2) { moveH(n, p & 1); if (p == 1) moveH(n, 0); }
    }
    RAW_BARRIER();
    if (p == 0) look(lv, mine0); else { moveH(12, p & 1); if (p == 1) moveH(12, 0); }
    RAW_BARRIER();
    issueB(4, false);
    if (p == 0) {
      asm volatile("s_waitcnt vmcnt(4)" : "+v"(lv));
      if (!__all(lv == tag)) poll(mine0);
      lsync[2] = 1u;
    } else {
#pragma unroll 1
      for (int sp = 0; sp < 64 * SPIN_MAX && lsync[2] == 0u; ++sp) {}
      asm volatile("" ::: "memory");
    }
    issueA(3); issueA(4);
    RAW_BARRIER();
    issueA(5); issueB(5, false);
    const u32 ta0 = sh0 + kv0 * 1024, ta1 = sh0 + kv1 * 1024, tbb = ((p < 2 ? kv0 : kv1) * 32 + 16 * (p & 1)) * 2048 + tn2 * 256 + kh * 128;
    u32 o0 = __builtin_amdgcn_readlane(ta0, 6), o1 = __builtin_amdgcn_readlane(ta1, 6), o4 = __builtin_amdgcn_readlane(tbb, 6);
    u32 o2 = o0 + 112 * 1024, o3 = o1 + 112 * 1024, o5 = o4 + 8 * 2048, o6 = o4 ^ 128, o7 = o6 + 8 * 2048;
    const u32 lbase = (u32)(size_t)lds + p * 4096;
    u32 la = lbase + 2 * G2_STAGE, tnx = 7;
    auto step = [&]() {
      asm volatile("s_mov_b32 m0, %8\n\ts_waitcnt vmcnt(16)\n\ts_barrier\n\t"
                   "buffer_load_dwordx4 %10, %11, %0 offen sc1 lds\n\ts_add_u32 m0, m0, 0x400\n\tv_readlane_b32 %0, %14, %9\n\t"
                   "buffer_load_dwordx4 %10, %11, %1 offen sc1 lds\n\ts_add_u32 m0, m0, 0x400\n\tv_readlane_b32 %1, %15, %9\n\t"
                   "buffer_load_dwordx4 %10, %11, %2 offen sc1 lds\n\ts_add_u32 m0, m0, 0x400\n\ts_lshl_b32 %8, %9, 14\n\t"
                   "buffer_load_dwordx4 %10, %11, %3 offen sc1 lds\n\ts_add_u32 m0, m0, 0xf400\n\ts_and_b32 %8, %8, 0xc000\n\t"
                   "buffer_load_dwordx4 %12, %13, %4 offen lds\n\ts_add_u32 m0, m0, 0x400\n\tv_readlane_b32 %4, %16, %9\n\t"
                   "buffer_load_dwordx4 %12, %13, %5 offen lds\n\ts_add_u32 m0, m0, 0x400\n\ts_add_u32 %2, %0, 0x1c000\n\ts_add_u32 %3, %1, 0x1c000\n\ts_add_u32 %8, %8, %17\n\ts_add_u32 %9, %9, 1\n\t"
                   "buffer_load_dwordx4 %12, %13, %6 offen lds\n\ts_add_u32 m0, m0, 0x400\n\ts_add_u32 %5, %4, 0x4000\n\ts_xor_b32 %6, %4, 0x80\n\t"
                   "buffer_load_dwordx4 %12, %13, %7 offen lds\n\ts_add_u32 %7, %6, 0x4000"
                   : "+s"(o0), "+s"(o1), "+s"(o2), "+s"(o3), "+s"(o4), "+s"(o5), "+s"(o6), "+s"(o7), "+s"(la), "+s"(tnx)
                   : "v"(l16), "s"(rw), "v"(vb0), "s"(rb2), "v"(ta0), "v"(ta1), "v"(tbb), "s"(lbase) : "memory", "scc");
    };
    asm volatile("s_nop 4");
    wait_vm<12>();
    step();
    if (p == 1) signal(1);
    step(); step();
    if (p == 0) look(lv1, mine1);
    step();
    if (p == 0) {
      asm volatile("s_waitcnt vmcnt(8)" : "+v"(lv1));
      if (!__all(lv1 == tag)) poll(mine1);
    }
#pragma unroll 1
    for (int t = 6; t < G2_NF - 4; ++t) step();
    LAS char* xs = lds + G2_X1 + p * 4096;
#pragma unroll
    for (int t = G2_NF - 4; t < G2_NT; ++t) {
      constexpr int NF = G2_NF, NT = G2_NT, XS = NF + 1;
      const int n2 = (t + 2 < NF) ? 8 : (t + 2 < NT ? 6 : 0), n3 = (t + 3 < NF) ? 8 : (t + 3 < NT ? 6 : 0);
      const int ex = ((t == XS + 1 || t == XS + 2) ? 4 : 0) + ((t > NT - 4 && t < NT - 1) ? 4 : 0);
      switch (n2 + n3 + ex) {
        case 16: wait_vm<16>(); break;
        case 14: wait_vm<14>(); break;
        case 12: wait_vm<12>(); break;
        case 10: wait_vm<10>(); break;
        case 4: wait_vm<4>(); break;
        default: wait_vm<0>(); break;
      }
      RAW_BARRIER();
      if (t == XS) {
#pragma unroll
        for (int i = 0; i < 4; ++i) hv[i] = *(LAS u32x4*)(xs + i * 1024 + l16);
#pragma unroll
        for (int i = 0; i < 4; ++i) st16<16>(rw, hv[i], l16, WS_XCH + (b * 4 + p) * 4096 + i * 1024);
        SB();
      }
      if (t + 4 < NT) { issueA(t + 4); issueB(t + 4, true); }
      if (t == NT - 4) {
#pragma unroll
        for (int i = 0; i < 4; ++i) dma16x<16>(rw, xs + i * 1024, l16, WS_XCH + (partner * 4 + p) * 4096 + i * 1024);
        SB();
      }
    }
    asm volatile(".Lk_main_end:");
    return;
  }

  __builtin_amdgcn_s_setprio(1);
  {
    const int t = tid, cls = wave;
    auto msync = [&](u32 k, u32 hk) {
      asm volatile("" ::: "memory");
      lsync[24 + wave] = k;
#pragma unroll 1
      for (int sp = 0; sp < 64 * SPIN_MAX && !__all(lsync[((lane & 4) ? 12 : 24) + (lane & 3)] >= ((lane & 4) ? hk : k)); ++sp) if (k == 1) __builtin_amdgcn_s_sleep(1);
      asm volatile("" ::: "memory");
    };
    __builtin_amdgcn_s_sleep(X_DELAY);
    u32x4 xw[4];
#pragma unroll
    for (int i = 0; i < 4; ++i) xw[i] = ld16<0>(mk_rsrc(x, S_ * D_ * 2), l16, (s2 + 256 * (2 * wave + (i >> 1))) * 2048 + (i & 1) * 1024);
    SB();
    make_tw(); make_mat();
    SB();
#pragma unroll
    for (int i = 0; i < 4; ++i) *(LAS u32x4*)(ldw + L_X + wave * 1024 + i * 256 + lane * 4) = xw[i];
    f32x2 v[2][8];
    float rs[2], nm[2];
#pragma unroll
    for (int i = 0; i < 16; ++i) v[i >> 3][i & 7] = bf_2(xw[i >> 2][i & 3]);
    wave_ln2(v, rs, nm);
    *(LAS f32x4*)(lds + G2_SYNC + 128 + wave * 16) = (f32x4){rs[0], nm[0], rs[1], nm[1]};
    msync(1, 0);
    u32x2 xr[8];
#pragma unroll
    for (int j = 0; j < 8; ++j) xr[j] = *(LAS u32x2*)(ldw + L_X + j * 512 + 2 * t);
    f32x4 stt[4];
#pragma unroll
    for (int j = 0; j < 4; ++j) stt[j] = *(LAS f32x4*)(lds + G2_SYNC + 128 + j * 16);
    u32x4 mre[2][2], mim[2][2];
    f32x4 tc[2][2], ts[2][2];
    auto consts32 = [&]() {
#pragma unroll
      for (int a = 0; a < 2; ++a)
#pragma unroll
        for (int T = 0; T < 2; ++T) {
          mre[a][T] = lds_read16(cb + (L_MAT - L_TW + (a * 2 + T) * 512) * 4);
          mim[a][T] = lds_read16(cb + (L_MAT - L_TW + (a * 2 + T) * 512 + 256) * 4);
          tc[a][T] = lds_readf(cb + ((a * 2 + T) * 512) * 4);
          ts[a][T] = lds_readf(cb + ((a * 2 + T) * 512 + 256) * 4);
        }
    };
    consts32();
    {
      u32x4 pm[2][4];
      u32 bad;
      auto request = [&]() {
#pragma unroll
        for (int vv = 0; vv < 2; ++vv)
#pragma unroll
          for (int i = 0; i < 4; ++i) pm[vv][i] = ld16<16>(rw, l16, WS_PART + i * 24576 + vv * 4096 + wave * 1024);
        lsync[8 + wave] = 1u;
      };
      f32x4 pr[4], pi[4];
      {
        f32x4 n[8];
#pragma unroll
        for (int j = 0; j < 8; ++j) n[j] = bf_4(xr[j][0], xr[j][1]) * stt[j >> 1][2 * (j & 1)] + stt[j >> 1][2 * (j & 1) + 1];
        const f32x4 a0 = n[0] + n[4], a1 = n[0] - n[4], a2 = n[2] + n[6], a3 = n[2] - n[6];
        const f32x4 a4 = n[1] + n[5], a5 = n[1] - n[5], a6 = n[3] + n[7], a7 = n[3] - n[7];
        const f32x4 b0 = a0 + a2, b2_ = a4 + a6, c0 = a5 - a7, c1 = a5 + a7;
        constexpr float RH = 0.70710678f;
        pr[0] = b0 + b2_;        pi[0] = b0 - b2_;
        pr[1] = a1 + c0 * RH;    pi[1] = -a3 - c1 * RH;
        pr[2] = a0 - a2;         pi[2] = a6 - a4;
        pr[3] = a1 - c0 * RH;    pi[3] = a3 - c1 * RH;
#pragma unroll
        for (int c = 0; c < 4; ++c) { PIN(pr[c]); PIN(pi[c]); }
      }
      SB();
      lds_wait(3, 2);
      get_tag();
      __builtin_amdgcn_s_sleep(G_DELAY);
      request();
      SB();
      const u32x4 tb = (u32x4){tag, tag >> 8, tag >> 16, tag >> 24} & 0xffu;
      auto check = [&]() {
        u32x4 o = {};
#pragma unroll
        for (int vv = 0; vv < 2; ++vv)
#pragma unroll
          for (int i = 0; i < 4; ++i) { pm[vv][i] ^= tb; o |= pm[vv][i]; }
        bad = ((o[0] | o[1]) | (o[2] | o[3])) & 0xffu;
      };
      check();
      if (__builtin_expect(__any(bad != 0u), 0)) {
        int sp = 0;
        do { asm volatile("" ::: "memory"); __builtin_amdgcn_s_sleep(4); request(); check(); } while (__any(bad != 0u) && ++sp < SPIN_MAX);
      }
      const f32x4 sa8 = sum_mod4(pm[0]) * 8.0f, g4 = round_bf4(sum_mod4(pm[1]) + 1.0f);
      pr[0] = pr[0] * g4 + sa8;
#pragma unroll
      for (int c = 0; c < 4; ++c) *(LAS u32x4*)(ldw + L_IMG + c * 1024 + ((4 * t) ^ ((t & 32) >> 1))) = pack4(c ? pr[c] * g4 : pr[c], pi[c] * g4);
    }
    msync(2, 1);
    if (b == 0 && tid == 0) __hip_atomic_store((GAS u32*)(ws + WS_EPOCH), tag, __ATOMIC_RELAXED, __HIP_MEMORY_SCOPE_AGENT);
    u32x4 po[4], voff;
    {
      u32x4 d[2][2], d2[2][2];
#pragma unroll
      for (int T = 0; T < 2; ++T) {
        LAS u32* src = ldw + L_IMG + cls * 1024 + 128 * q + xl + 16 * (T ^ (q & 1));
#pragma unroll
        for (int V = 0; V < 2; ++V) d[T][V] = (u32x4){src[512 * V], src[512 * V + 32], src[512 * V + 64], src[512 * V + 96]};
      }
      voff = *(LAS u32x4*)(ldw + L_ADR + (cls ? 256 : 0) + lane * 4);
      f32x4 re[2][2], im[2][2];
      SB();
#pragma unroll
      for (int hf = 0; hf < 2; ++hf)
#pragma unroll
        for (int T = 0; T < 2; ++T) {
          re[hf][T] = mfma16(d[T][1], mre[hf][1], mfma16(d[T][0], mre[hf][0], z));
          im[hf][T] = mfma16(d[T][1], mim[hf][1], mfma16(d[T][0], mim[hf][0], z));
        }
      SB();
#pragma unroll
      for (int hf = 0; hf < 2; ++hf)
#pragma unroll
        for (int T = 0; T < 2; ++T) d2[hf][T] = cmul_pack<false>(re[hf][T], im[hf][T], tc[hf][T], ts[hf][T]);
      SB();
#pragma unroll
      for (int hf = 0; hf < 2; ++hf)
#pragma unroll
        for (int tp = 0; tp < 2; ++tp) {
          re[hf][tp] = mfma16(mre[tp][1], d2[hf][1], mfma16(mre[tp][0], d2[hf][0], z));
          im[hf][tp] = mfma16(mim[tp][1], d2[hf][1], mfma16(mim[tp][0], d2[hf][0], z));
        }
      SB();
      if (__builtin_expect(!__all(lsync[12 + (lane & 3)] >= 2u), 0)) lds_wait4(12, 2);
#pragma unroll
      for (int i = 0; i < 4; ++i) {
        u32x4 o = pack4(re[i >> 1][i & 1], im[i >> 1][i & 1]);
        if (i == 1) o[0] = swap_lane0_32(o[0], t);
        po[i] = no_sent(o);
        st16<16>(rw, po[i], voff[i], WS_P1 + cls * (1 << 20) + s2 * 64);
      }
    }
    SB();
    {
      Fft2Consts k;
      fft2_consts(k, cb);
      lsync[8 + wave] = 2u;
      SB();
      wait_vm<0>();
      __builtin_amdgcn_s_sleep(XD1);
      u32x4 r[4];
#pragma unroll
      for (int i = 0; i < 4; ++i) r[i] = ld16<16>(rw, l16, WS_P1 + tileid * 16384 + wave * 4096 + i * 1024);
      RETRY4(umax4(r[i]) == SENT, r[i] = ld16<16>(rw, l16, WS_P1 + tileid * 16384 + wave * 4096 + i * 1024));
      u32x4 wl[4];
#pragma unroll
      for (int i = 0; i < 4; ++i) wl[i] = ld16<16>(rw, l16, WS_WT + tileid * 16384 + wave * 4096 + i * 1024);
      SB();
      LAS u32x4* stg = (LAS u32x4*)(ldw + L_DAT) + (lane & 3) * 256 + wave * 16 + ((lane >> 2) ^ ((lane & 3) << 1));
#pragma unroll
      for (int e = 0; e < 4; ++e) stg[e * 64] = (u32x4){r[0][e], r[1][e], r[2][e], r[3][e]};
      msync(3, 0);
      auto getw = [&](u32x4 (&wp)[4]) {
#pragma unroll
        for (int i = 0; i < 4; ++i) *(LAS u32x4*)(ldw + L_W + (wave * 4 + i) * 256 + lane * 4) = wl[i];
        msync(4, 0);
#pragma unroll
        for (int c = 0; c < 4; ++c) wp[c] = *(LAS u32x4*)(ldw + L_W + lane * 64 + (((4 * wave + c) ^ xl) << 2));
      };
      if (cls2 != 0) {
        LAS u32x4* dp = colp(4 * wave);
        u32x4 d[4];
#pragma unroll
        for (int c = 0; c < 4; ++c) d[c] = dp[c * 64];
        f32x4 re[4], im[4];
#pragma unroll
        for (int c = 0; c < 4; ++c) { re[c] = mfma16(d[c], k.mt[1][0], z); im[c] = mfma16(d[c], k.mt[1][1], z); }
        fft2_tail<4, false>(re, im, k, getw);
#pragma unroll
        for (int c = 0; c < 4; ++c) dp[c * 64] = pack4(re[c], im[c]);
      } else {
        const int m0 = 7 + (tile >= 62 ? 1 : 0) - 2 * wave;
        LAS u32x4* const pa[2] = {colp(2 * wave), colp(2 * wave + 1)};
        LAS u32x4* const pmr[2] = {colp((tile == 62 && wave == 0) ? 0 : 8 + (m0 & 7)), colp(8 + ((m0 - 1) & 7))};
        fft2_packed<2>(pa, pmr, k, getw);
      }
      msync(5, 3);
      u32x4 oc[4];
#pragma unroll
      for (int e = 0; e < 4; ++e) oc[e] = stg[e * 64];
#pragma unroll
      for (int i = 0; i < 4; ++i) {
        u32x4 o;
#pragma unroll
        for (int e = 0; e < 4; ++e) o[e] = max(oc[e][i] ^ r[i][e], 1u) ^ r[i][e];
        st16<16>(rw, o, l16, WS_P1 + tileid * 16384 + wave * 4096 + i * 1024);
      }
    }
    lsync[8 + wave] = 3u;
    SB();
    consts32();
    SB();
    f32x4 xv[8];
#pragma unroll
    for (int j = 0; j < 8; ++j) { xv[j] = bf_4(xr[j][0], xr[j][1]); PIN(xv[j]); }
    SB();
    wait_vm<0>();
    __builtin_amdgcn_s_sleep(XD2);
    u32x4 d[4];
#pragma unroll
    for (int i = 0; i < 4; ++i) d[i] = ld16<16>(rw, voff[i], WS_P1 + cls * (1 << 20) + s2 * 64);
    lsync[8 + wave] = 4u;
    RETRY4(umin4(d[i] ^ po[i]) == 0u, d[i] = ld16<16>(rw, voff[i], WS_P1 + cls * (1 << 20) + s2 * 64));
    lsync[8 + wave] = 5u;
    d[1][0] = swap_lane0_32(d[1][0], t);
    {
      f32x4 re[2][2], im[2][2];
      u32x4 d2[2][2];
      SB();
#pragma unroll
      for (int hf = 0; hf < 2; ++hf)
#pragma unroll
        for (int T = 0; T < 2; ++T) {
          re[hf][T] = mfma16(d[2 * T + 1], mre[hf][1], mfma16(d[2 * T], mre[hf][0], z));
          im[hf][T] = mfma16(d[2 * T + 1], mim[hf][1], mfma16(d[2 * T], mim[hf][0], z));
        }
      SB();
#pragma unroll
      for (int hf = 0; hf < 2; ++hf)
#pragma unroll
        for (int T = 0; T < 2; ++T) d2[hf][T] = cmul_pack<false>(re[hf][T], im[hf][T], tc[hf][T], ts[hf][T]);
      SB();
#pragma unroll
      for (int hf = 0; hf < 2; ++hf)
#pragma unroll
        for (int tp = 0; tp < 2; ++tp) {
          re[hf][tp] = mfma16(d2[hf][1], mre[tp][1], mfma16(d2[hf][0], mre[tp][0], z));
          im[hf][tp] = mfma16(d2[hf][1], mim[tp][1], mfma16(d2[hf][0], mim[tp][0], z));
        }
      SB();
#pragma unroll
      for (int hf = 0; hf < 2; ++hf)
#pragma unroll
        for (int tp = 0; tp < 2; ++tp) {
          const int off = cls * 1024 + 32 * (16 * tp + xl) + 4 * ((4 * hf + q) ^ (xl & 7));
          *(LAS f32x4*)(ldf + L_PL2 + off) = re[hf][tp];
          *(LAS f32x4*)(ldf + L_IMG + off) = im[hf][tp];
        }
    }
    msync(6, 4);
    f32x4 ur[4], ui[4];
    {
      const int off = 4 * (t ^ ((t >> 3) & 7));
#pragma unroll
      for (int c = 0; c < 4; ++c) { ur[c] = *(LAS f32x4*)(ldf + L_IMG + c * 1024 + off); ui[c] = *(LAS f32x4*)(ldf + L_PL2 + c * 1024 + off); }
    }
    const f32x4 ga = *(LAS f32x4*)(ldf + L_MOD + 2048 + 4 * t);
    f32x4 gv[4], sw[4];
#pragma unroll
    for (int i = 0; i < 4; ++i) { sw[i] = lds_readf(cb + (L_MOD - L_TW + i * 256) * 4); gv[i] = lds_readf(cb + (L_MOD - L_TW + 1024 + i * 256) * 4); }
    {
      const f32x4 e0 = ur[0] + ui[0], e1 = ur[0] - ui[0];
      const f32x4 p0 = e0 + 2.0f * ur[2], p2 = e0 - 2.0f * ur[2], p1 = e1 - 2.0f * ui[2], p3 = e1 + 2.0f * ui[2];
      const f32x4 q0 = ur[1] + ur[3], q2 = ui[3] - ui[1], A = ur[1] - ur[3], B = ui[1] + ui[3];
      const f32x4 q1 = A - B, q3 = A + B;
      constexpr float R2 = 1.41421356f;
      const f32x4 y[8] = {p0 + 2.0f * q0, p1 + R2 * q1, p2 + 2.0f * q2, p3 - R2 * q3, p0 - 2.0f * q0, p1 - R2 * q1, p2 - 2.0f * q2, p3 + R2 * q3};
#pragma unroll
      for (int j = 0; j < 8; ++j) {
        const f32x4 v = xv[j] + ga * y[j];
        *(LAS u32x2*)(ldw + L_DAT + j * 512 + 2 * t) = (u32x2){pk_bf16(v[0], v[1]), pk_bf16(v[2], v[3])};
      }
    }
    msync(7, 0);
#pragma unroll
    for (int i = 0; i < 4; ++i) xw[i] = *(LAS u32x4*)(ldw + L_DAT + wave * 1024 + i * 256 + lane * 4);
#pragma unroll
    for (int i = 0; i < 4; ++i) st16<16>(ro, xw[i], l16, (s2 + 256 * (2 * wave + (i >> 1))) * 2048 + (i & 1) * 1024);
#pragma unroll
    for (int i = 0; i < 16; ++i) v[i >> 3][i & 7] = bf_2(xw[i >> 2][i & 3]);
    wave_ln2(v, rs, nm);
#pragma unroll
    for (int r = 0; r < 2; ++r)
#pragma unroll
      for (int i = 0; i < 8; ++i) {
        const f32x2 gg = (i & 1) ? gv[i >> 1].zw : gv[i >> 1].xy, s = (i & 1) ? sw[i >> 1].zw : sw[i >> 1].xy;
        v[r][i] = (v[r][i] * rs[r] + nm[r]) * gg + s;
      }
    wave_ln2(v, rs, nm);
#pragma unroll
    for (int i = 0; i < 4; ++i) {
      const float a = rs[i >> 1] * GELU_A, c = nm[i >> 1] * GELU_A;
      u32x4 o;
#pragma unroll
      for (int e = 0; e < 4; ++e) { const f32x2 r = v[i >> 1][4 * (i & 1) + e] * a + c; o[e] = pk_bf16(r.x, r.y); }
      st16<16>(rw, o, l16, WS_Y2 + (s2 * 8 + 2 * wave) * 2048 + i * 1024);
    }
    lsync[8 + wave] = 6u;
    SB();
  }
  const int wm_ = wave >> 1, wn = wave & 1, fr = xl;
  const u32 l0 = (u32)(size_t)lds;
  const u32 bbase = l0 + (lane >> 5) * 1024 + ((lane & 31) >> 2) * 128 + (lane & 3) * 8, bf_ = (lane & 31) >> 3;
  {
    u32 boff[7], aoff[2];
    int ncol[7];
#pragma unroll
    for (int j = 0; j < 4; ++j) { boff[j] = bbase + G1_BOFF + wn * 4096 + ((j ^ bf_) << 5); ncol[j] = gcol + wn * 128 + 16 * j; }
#pragma unroll
    for (int j = 0; j < 2; ++j) { boff[4 + j] = bbase + G1_BOFF + 2048 + (((2 * wn + j) ^ bf_) << 5); ncol[4 + j] = gcol + 64 + 32 * wn + 16 * j; }
    boff[6] = l0 + G1_BOFF + 6144 + (lane >> 2) * 64 + ((wn ^ (q & 1)) << 5) + (lane & 3) * 8;
    ncol[6] = hcol + 16 * wn;
    aoff[0] = l0 + (wm_ * 64 + fr) * 128 + ((q ^ (fr >> 1)) << 4);
    aoff[1] = l0 + (wm_ * 64 + fr) * 128 + ((q ^ (fr >> 1) ^ 4) << 4);
    u32x2 bv[7];
#pragma unroll
    for (int j = 0; j < 7; ++j) bv[j] = *(LAS u32x2*)(lds + L_B1 + (ncol[j] - col0 + 4 * q) * 2);
    SB();
    f32x4 acc[4][7];
#pragma unroll
    for (int j = 0; j < 7; ++j) {
      const f32x4 bf = bf_4(bv[j][0], bv[j][1]) * GELU_A;
#pragma unroll
      for (int i = 0; i < 4; ++i) { acc[i][j] = bf; PIN(acc[i][j]); }
      PIN(boff[j]);
    }
    PIN(aoff[0]); PIN(aoff[1]);
    u32x4 fa[4][2], fb[7][2];
    u32x2 blo;
    auto rdA = [&](int i, int s) { fa[i][s] = lds_read16(aoff[s] + i * 2048); SB(); };
    auto rdB = [&](int j, int jd, int s, int h) {
      const u32x2 v = tr_read(boff[j] + (2 * s + h) * 7168);
      if (h) fb[jd][s] = (u32x4){blo[0], blo[1], v[0], v[1]}; else blo = v;
      SB();
    };
    auto ldA = [&](int i) { rdA(i, 0); rdA(i, 1); };
    auto ldB = [&](int j, int s) { rdB(j, j, s, 0); rdB(j, j, s, 1); };
    wait_vm<0>();
    lsync[24 + wave] = 8u;
    RAW_BARRIER();
    RAW_BARRIER();
    ldB(0, 0); ldA(0); ldB(0, 1); ldB(1, 1); ldB(1, 0); ldA(1); ldA(2);
#pragma unroll
    for (int j = 2; j < 5; ++j) { ldB(j, 0); ldB(j, 1); }
    int slot = 0;
    for (int t = 0; t < G1_NT; ++t) {
      const int delta = (slot == 2) ? -2 * G1_BST : G1_BST, deltaA = (slot == 2) ? -32768 : 16384;
      slot = (slot == 2) ? 0 : slot + 1;
      PA(0, 0, 0, 3);
      PB(0, 1, 1, 5, 0);
      PB(1, 1, 0, 5, 1);
      PB(1, 0, 1, 6, 0);
      PB(2, 0, 0, 6, 1);
      PU(2, 1, 1, boff[0], boff[1]);
      PU(3, 1, 0, boff[2], boff[3]);
      PU(3, 0, 1, boff[4], boff[5]);
      MM(3, 2, 0); UPD(aoff[0], deltaA); MM(3, 2, 1); UPD(aoff[1], deltaA);
      MM(2, 2, 1); UP(boff[6]); MM(2, 2, 0);
      PP(1, 2, 0); PP(0, 2, 1);
      __builtin_amdgcn_s_waitcnt(0xC07F);
      RAW_BARRIER(); SB();
      PB(0, 3, 0, 0, 0);
      PB(1, 3, 1, 0, 1);
      PB(2, 3, 0, 1, 0);
      PB(3, 3, 1, 1, 1);
      PB(3, 4, 0, 2, 0);
      PB(2, 4, 1, 2, 1);
      PB(1, 4, 0, 3, 0);
      PB(0, 4, 1, 3, 1);
      PB(0, 5, 0, 4, 0);
      PB(0, 6, 1, 4, 1);
      PA(1, 6, 0, 0);
      PP(1, 5, 1);
      PA(2, 5, 0, 1);
      PP(2, 6, 1);
      PA(3, 6, 0, 2);
      PP(3, 5, 1);
    }
    LAS char* hl = lds + wm_ * 8192 + wn * 1024 + l16;
    u32 o[8];
    MFMA_DRAIN();
#pragma unroll
    for (int n = 0; n < 12; n += 2) {
      const int pj = n >> 2, i = n & 3;
      gelu16(acc[i][2 * pj], acc[i][2 * pj + 1], acc[i + 1][2 * pj], acc[i + 1][2 * pj + 1], o);
      *(volatile LAS u32x4*)(hl + pj * G2_STAGE + i * 2048) = (u32x4){o[0], o[1], o[2], o[3]};
      *(volatile LAS u32x4*)(hl + pj * G2_STAGE + i * 2048 + 2048) = (u32x4){o[4], o[5], o[6], o[7]};
      lsync[4 + wave] = n + 2;
      SB();
    }
    gelu16(acc[0][6], acc[1][6], acc[2][6], acc[3][6], o);
#pragma unroll
    for (int i = 0; i < 2; ++i) {
      auto s0 = __builtin_amdgcn_permlane16_swap(o[4 * i], o[4 * i + 2], false, false);
      auto s1 = __builtin_amdgcn_permlane16_swap(o[4 * i + 1], o[4 * i + 3], false, false);
      *(LAS u32x4*)(lds + G2_HH + (wave * 2 + i) * 1024 + l16) = (u32x4){(u32)s0[0], (u32)s1[0], (u32)s0[1], (u32)s1[1]};
    }
    SB();
  }
  u32 boff[4], aoff;
#pragma unroll
  for (int j = 0; j < 4; ++j) { boff[j] = bbase + G2_BOFF + (j >> 1) * 2048 + (((2 * wn + (j & 1)) ^ bf_) << 5); PIN(boff[j]); }
  aoff = l0 + wm_ * 8192 + l16;
  PIN(aoff);
  f32x4 acc[4][4];
#pragma unroll
  for (int j = 0; j < 4; ++j)
#pragma unroll
    for (int i = 0; i < 4; ++i) { acc[i][j] = z; PIN(acc[i][j]); }
  u32x4 fa[4][2], fb[4][2];
  u32x2 blo;
  auto rdA = [&](int i, int s) { fa[i][s] = lds_read16(aoff + i * 2048 + s * 1024); SB(); };
  auto rdB = [&](int j, int jd, int s, int h) {
    const u32x2 v = tr_read(boff[j] + (2 * s + h) * 4096);
    if (h) fb[jd][s] = (u32x4){blo[0], blo[1], v[0], v[1]}; else blo = v;
    SB();
  };
  auto ldA = [&](int i) { rdA(i, 0); rdA(i, 1); };
  auto ldB = [&](int j, int s) { rdB(j, j, s, 0); rdB(j, j, s, 1); };
  auto step_sync = [&]() { __builtin_amdgcn_s_waitcnt(0xC07F); RAW_BARRIER(); SB(); };
  step_sync();
  ldB(0, 0); ldA(0); ldB(0, 1); ldB(1, 1); ldB(1, 0); ldA(1); ldA(2);
  for (int t = 0; t < G2_NF; ++t) {
    const int delta = ((t & 3) == 3) ? -3 * G2_STAGE : G2_STAGE;
    PA(0, 0, 0, 3);
    PB(0, 1, 1, 2, 0);
    PB(1, 1, 0, 2, 1);
    PB(1, 0, 1, 3, 1);
    PB(2, 0, 0, 3, 0);
    PU(2, 1, 1, boff[0], boff[1]);
    PU(3, 1, 0, boff[3], boff[2]);
    MM(3, 0, 1); UP(aoff); MM(3, 0, 0);
    step_sync();
    PB(0, 2, 0, 0, 0);
    PB(0, 3, 1, 0, 1);
    PA(1, 3, 0, 0);
    PB(1, 2, 1, 1, 1);
    PA(2, 2, 0, 1);
    PB(2, 3, 1, 1, 0);
    PA(3, 3, 0, 2);
    PP(3, 2, 1);
  }
  LAS u32x4* xs = (LAS u32x4*)(lds + G2_X1 + wave * 4096) + lane;
  u32x4 xr[4], px[4];
  ldA(3);
#pragma unroll
  for (int i = 0; i < 4; ++i) xr[i] = xs[i * 64];
  LAS f32x4* gl = (LAS f32x4*)(lds + L_GB) + wn * 8 + (q & 1) * 4 + (q >> 1) * 2;
  const f32x4 gb[2] = {gl[0], gl[1]}, bb[2] = {gl[16], gl[17]};
  SB();
  f32x4 xb[4][2];
  auto give = [&](int i, int h) {
    px[i][2 * h] = min(pk_bf16(acc[i][2 + h][0], acc[i][2 + h][1]), SENT - 1);
    px[i][2 * h + 1] = min(pk_bf16(acc[i][2 + h][2], acc[i][2 + h][3]), SENT - 1);
    if (h) xs[i * 64] = px[i];
    SB();
  };
  u32 ooff = ((tm * 16 + wm_ * 8 + (fr >> 3)) + 256 * (fr & 7)) * 2048 + (q & 1) * 32 + (q >> 1) * 16;
  PIN(ooff);
  auto fin = [&](int i) {
    f32x4 s[2];
#pragma unroll
    for (int j = 0; j < 2; ++j) s[j] = acc[i][j] + bf_4(px[i][2 * j], px[i][2 * j + 1]);
#pragma unroll
    for (int e = 0; e < 4; ++e) {
      const float a0 = s[0][e], a1 = s[1][e];
      auto w = __builtin_amdgcn_permlane16_swap(__builtin_bit_cast(u32, a0), __builtin_bit_cast(u32, a1), false, false);
      s[0][e] = __builtin_bit_cast(float, (u32)w[0]); s[1][e] = __builtin_bit_cast(float, (u32)w[1]);
    }
    const f32x4 r0 = xb[i][0] + gb[0] * s[0], r1 = xb[i][1] + gb[1] * s[1];
    st16<16>(ro, (u32x4){pk_bf16(r0[0], r0[1]), pk_bf16(r0[2], r0[3]), pk_bf16(r1[0], r1[1]), pk_bf16(r1[2], r1[3])}, ooff,
             i * 4096 + (tn2 * 128 + kh * 64 + wn * 32) * 2);
    SB();
  };
#define MH(i, j, f) MFMA_IP(acc[i][j], fb[o + (j)][f], fa[i][f])
#define GV(i, h) do { if (t == G2_NF) give(i, h); } while (0)
#pragma unroll
  for (int t = G2_NF; t < G2_NT; ++t) {
    const int delta = ((t & 3) == 3) ? -3 * G2_STAGE : G2_STAGE;
    const int o = 2 * ((t - G2_NF) & 1), on = 2 - o;
    if (t > G2_NF + 1 && t <= G2_NF + 5) {
      const int i = t - G2_NF - 2;
#pragma unroll
      for (int j = 0; j < 2; ++j) { xb[i][j] = bb[j] + bf_4(xr[i][2 * j], xr[i][2 * j + 1]); PIN(xb[i][j]); }
      SB();
    }
    if (t == G2_NT - 1) {
      step_sync();
#pragma unroll
      for (int i = 0; i < 4; ++i) px[i] = xs[i * 64];
      SB();
    }
    MH(0, 0, 0); UP(boff[0]); MH(0, 0, 1); UP(boff[1]); MH(0, 1, 1); UP(aoff); MH(0, 1, 0);
    if (t + 1 < G2_NT) {
      step_sync();
      MH(1, 1, 0); rdB(0, on, 0, 0); rdB(0, on, 0, 1); MH(1, 1, 1); rdA(0, 0); rdA(0, 1);
      MH(1, 0, 1); rdB(0, on, 1, 0); rdB(0, on, 1, 1); MH(1, 0, 0); rdB(1, on + 1, 1, 0); rdB(1, on + 1, 1, 1);
      MH(2, 0, 0); rdB(1, on + 1, 0, 0); GV(0, 0); MH(2, 0, 1); rdB(1, on + 1, 0, 1); GV(0, 1);
      MH(2, 1, 1); rdA(1, 0); GV(1, 0); MH(2, 1, 0); rdA(1, 1); GV(1, 1);
      MH(3, 1, 0); rdA(2, 0); GV(2, 0); MH(3, 1, 1); rdA(2, 1); GV(2, 1);
      MH(3, 0, 1); rdA(3, 1); GV(3, 0); MH(3, 0, 0); rdA(3, 0); GV(3, 1);
    } else {
      MH(1, 1, 0); MH(1, 1, 1); MH(1, 0, 1); MH(1, 0, 0);
      RETRY4(umax4(px[i]) == SENT, px[i] = ld16<16>(rw, l16, WS_XCH + (partner * 4 + wave) * 4096 + i * 1024));
      SB();
      MH(2, 0, 0); MH(2, 0, 1); MH(2, 1, 1); MH(2, 1, 0);
      fin(0);
      MH(3, 1, 0); MH(3, 1, 1); MH(3, 0, 1); MH(3, 0, 0);
      fin(1); fin(2); fin(3);
    }
  }
}

extern "C" void kernel_launch(void* const* d_in, const int* in_sizes, int n_in, void* d_out, int out_size, void* d_ws, size_t ws_size,
                              hipStream_t stream) {
  const u16* x = (const u16*)d_in[0];
  const u16* wr = (const u16*)d_in[1];
  const u16* wi = (const u16*)d_in[2];
  const u16* w1 = (const u16*)d_in[4];
  const u16* b1 = (const u16*)d_in[5];
  const u16* w2 = (const u16*)d_in[6];
  const u16* b2 = (const u16*)d_in[7];
  const u16* c = (const u16*)d_in[8];
  const u16* wm = (const u16*)d_in[9];
  char* ws = (char*)d_ws;
  (void)hipFuncSetAttribute((const void*)k_main, hipFuncAttributeMaxDynamicSharedMemorySize, MAIN_LDS);
  k_main<<<256, 512, MAIN_LDS, stream>>>(Args{x, wr, wi, c, wm, w1, b1, w2, b2, ws, (u16*)d_out});
}
```
